```python
import torch

def _softmax(x, axis=-1):
    return torch.softmax(x, dim=axis)
def _gelu(x):
    return torch.nn.functional.gelu(x, approximate="tanh")
def _silu(x):
    return torch.nn.functional.silu(x)
def _sigmoid(x):
    return torch.sigmoid(x)
def _layernorm(x, axis=-1, eps=1e-5):
    m = x.mean(dim=axis, keepdim=True)
    v = x.var(dim=axis, keepdim=True, unbiased=False)
    return (x - m) / torch.sqrt(v + eps)
def _scatter_add(tgt, idx, src, axis=0):
    return tgt.clone().index_add_(axis, idx, src)
def _segment_sum(src, seg, n):
    out = torch.zeros((n,) + src.shape[1:], dtype=src.dtype)
    return out.index_add_(0, seg, src)
def _pad(x, pw):
    flat = tuple(v for pair in reversed(pw) for v in pair)
    return torch.nn.functional.pad(x, flat)

kernel_name = 'gated_mix__linear_kernel_hedgehog__linear_kernel_hedgehog__prompt'

def setup_inputs():

    _w = (lambda a: (lambda t: t.to(torch.bfloat16) if t.is_floating_point() else t)(torch.as_tensor(a)))
    def _A_setup():
        B, S, D, H = 1, 2048, 576, 32
        Dh = D // H
        return {
            "x": _w((lambda *s: torch.randn(*s, dtype=torch.bfloat16) * 0.05)(B, S, D)),
            "wqkv": _w((lambda *s: torch.randn(*s, dtype=torch.bfloat16) * 0.8)(D, 3 * D)),
            "w_phi": _w((lambda *s: torch.randn(*s, dtype=torch.bfloat16) * 0.8)(H, Dh, Dh) * (Dh ** -0.5)),
            "wo": _w((lambda *s: torch.randn(*s, dtype=torch.bfloat16) * 0.05)(D, D)),
        }
    _A_in = _A_setup()

    def _B_setup():
        B, S, D, H = 1, 2048, 576, 32
        Dh = D // H
        return {
            "x": _w((lambda *s: torch.randn(*s, dtype=torch.bfloat16) * 0.05)(B, S, D)),
            "wqkv": _w((lambda *s: torch.randn(*s, dtype=torch.bfloat16) * 0.8)(D, 3 * D)),
            "w_phi": _w((lambda *s: torch.randn(*s, dtype=torch.bfloat16) * 0.8)(H, Dh, Dh) * (Dh ** -0.5)),
            "wo": _w((lambda *s: torch.randn(*s, dtype=torch.bfloat16) * 0.05)(D, D)),
        }
    _B_in = _B_setup()

    return {
        "A_x": _A_in["x"],
        "A_wqkv": _A_in["wqkv"],
        "A_w_phi": _A_in["w_phi"],
        "A_wo": _A_in["wo"],
        "B_x": _B_in["x"],
        "B_wqkv": _B_in["wqkv"],
        "B_w_phi": _B_in["w_phi"],
        "B_wo": _B_in["wo"],
        "gate_w": _w((lambda *s: torch.randn(*s, dtype=torch.bfloat16) * 0.05)(576) * 16.0),
    }

def reference(A_x, A_wqkv, A_w_phi, A_wo, B_x, B_wqkv, B_w_phi, B_wo, gate_w):
    def _A_ref(x, wqkv, w_phi, wo):
        B, S, D = x.shape
        H = 32; Dh = D // H
        qkv = (lambda x, *d: x.permute(*d))((x @ wqkv).reshape(B, S, 3, H, Dh), 2, 0, 3, 1, 4)
        q, k, v = qkv[0], qkv[1], qkv[2]
        pq = torch.einsum("bhsd,hdf->bhsf", q, w_phi)
        pk = torch.einsum("bhsd,hdf->bhsf", k, w_phi)
        phi_q = torch.concatenate([_softmax(pq, axis=-1), _softmax(-pq, axis=-1)], axis=-1)
        phi_k = torch.concatenate([_softmax(pk, axis=-1), _softmax(-pk, axis=-1)], axis=-1)
        kv = torch.einsum("bhsf,bhsd->bhfd", phi_k, v)
        z  = torch.einsum("bhsf->bhf", phi_k)
        num = torch.einsum("bhqf,bhfd->bhqd", phi_q, kv)
        den = torch.einsum("bhqf,bhf->bhq", phi_q, z)[..., None] + 1e-6
        o = num / den
        return (lambda x, *d: x.permute(*d))(o, 0, 2, 1, 3).reshape(B, S, D) @ wo

    def _B_ref(x, wqkv, w_phi, wo):
        B, S, D = x.shape
        H = 32; Dh = D // H
        qkv = (lambda x, *d: x.permute(*d))((x @ wqkv).reshape(B, S, 3, H, Dh), 2, 0, 3, 1, 4)
        q, k, v = qkv[0], qkv[1], qkv[2]
        pq = torch.einsum("bhsd,hdf->bhsf", q, w_phi)
        pk = torch.einsum("bhsd,hdf->bhsf", k, w_phi)
        phi_q = torch.concatenate([_softmax(pq, axis=-1), _softmax(-pq, axis=-1)], axis=-1)
        phi_k = torch.concatenate([_softmax(pk, axis=-1), _softmax(-pk, axis=-1)], axis=-1)
        kv = torch.einsum("bhsf,bhsd->bhfd", phi_k, v)
        z  = torch.einsum("bhsf->bhf", phi_k)
        num = torch.einsum("bhqf,bhfd->bhqd", phi_q, kv)
        den = torch.einsum("bhqf,bhf->bhq", phi_q, z)[..., None] + 1e-6
        o = num / den
        return (lambda x, *d: x.permute(*d))(o, 0, 2, 1, 3).reshape(B, S, D) @ wo

    x = A_x
    g = _sigmoid((x * gate_w).sum(-1, keepdims=True))
    a = _A_ref(x, A_wqkv, A_w_phi, A_wo)
    b = _B_ref(x, B_wqkv, B_w_phi, B_wo)
    return g * a + (1 - g) * b
```

```cpp
#include <hip/hip_runtime.h>
#include <hip/hip_bf16.h>
#include <stdint.h>

typedef __hip_bfloat16 bf16;
static __device__ __forceinline__ float b2f(bf16 v) { return __bfloat162float(v); }
static __device__ __forceinline__ float rbf(float v) { return __bfloat162float(__float2bfloat16(v)); }

constexpr int S = 2048, D = 576, H = 32, DH = 18, N3 = 1728;

__global__ void k_qkv(const bf16* x, const bf16* wa, const bf16* wb, float* qkv) {
    int n = blockIdx.x * 64 + (threadIdx.x & 63);
    int s0 = blockIdx.y * 16 + (threadIdx.x >> 6) * 4;
    int br = blockIdx.z;
    const bf16* w = br ? wb : wa;
    float acc[4] = {0, 0, 0, 0};
    for (int k = 0; k < D; ++k) {
        float wv = b2f(w[(size_t)k * N3 + n]);
        for (int i = 0; i < 4; ++i) acc[i] += b2f(x[(size_t)(s0 + i) * D + k]) * wv;
    }
    for (int i = 0; i < 4; ++i) qkv[((size_t)br * S + s0 + i) * N3 + n] = rbf(acc[i]);
}
__global__ void k_phi(const float* qkv, const bf16* wpa, const bf16* wpb, float* phi) {
    int idx = blockIdx.x * blockDim.x + threadIdx.x;
    int h = idx & 31, s = (idx >> 5) & 2047, qk = (idx >> 16) & 1, br = idx >> 17;
    const bf16* wp = (br ? wpb : wpa) + h * DH * DH;
    const float* q = qkv + ((size_t)br * S + s) * N3 + qk * D + h * DH;
    float p[DH];
    for (int f = 0; f < DH; ++f) { float a = 0; for (int d = 0; d < DH; ++d) a += q[d] * b2f(wp[d * DH + f]); p[f] = rbf(a); }
    float mx = p[0], mn = p[0];
    for (int f = 1; f < DH; ++f) { mx = fmaxf(mx, p[f]); mn = fminf(mn, p[f]); }
    float sp = 0, sn = 0;
    for (int f = 0; f < DH; ++f) { sp += __expf(p[f] - mx); sn += __expf(mn - p[f]); }
    float* o = phi + (size_t)idx * 36;
    for (int f = 0; f < DH; ++f) { o[f] = rbf(__expf(p[f] - mx) / sp); o[DH + f] = rbf(__expf(mn - p[f]) / sn); }
}
__global__ void k_kv(const float* qkv, const float* phi, float* kv) {
    int bh = blockIdx.x; int br = bh >> 5, h = bh & 31;
    int t = threadIdx.x;
    if (t >= 36 * 19) return;
    int f = t / 19, d = t % 19;
    float a = 0;
    for (int s = 0; s < S; ++s) {
        float pk = phi[((((size_t)br * 2 + 1) * S + s) * H + h) * 36 + f];
        float v = d < 18 ? qkv[((size_t)br * S + s) * N3 + 2 * D + h * DH + d] : 1.f;
        a += pk * v;
    }
    kv[(size_t)bh * 684 + t] = rbf(a);
}
__global__ void k_o(const float* phi, const float* kv, float* o) {
    int idx = blockIdx.x * blockDim.x + threadIdx.x;
    int h = idx & 31, s = (idx >> 5) & 2047, br = idx >> 16;
    const float* pq = phi + ((((size_t)br * 2 + 0) * S + s) * H + h) * 36;
    const float* k = kv + (size_t)(br * 32 + h) * 684;
    float den = 0;
    for (int f = 0; f < 36; ++f) den += pq[f] * k[f * 19 + 18];
    den = rbf(rbf(den) + 1e-6f);
    for (int d = 0; d < DH; ++d) {
        float n = 0;
        for (int f = 0; f < 36; ++f) n += pq[f] * k[f * 19 + d];
        o[((size_t)br * S + s) * D + h * DH + d] = rbf(rbf(n) / den);
    }
}
__global__ void k_out(const float* o, const bf16* woa, const bf16* wob, const bf16* x, const bf16* gw, bf16* out) {
    int s = blockIdx.x; int n = threadIdx.x;
    __shared__ float red[576];
    red[n] = rbf(b2f(x[(size_t)s * D + n]) * b2f(gw[n]));
    __syncthreads();
    float l = 0; for (int k = 0; k < D; ++k) l += red[k];
    l = rbf(l);
    float g = rbf(1.f / (1.f + __expf(-l)));
    float a = 0, b = 0;
    for (int k = 0; k < D; ++k) { a += o[(size_t)s * D + k] * b2f(woa[(size_t)k * D + n]); b += o[((size_t)S + s) * D + k] * b2f(wob[(size_t)k * D + n]); }
    a = rbf(a); b = rbf(b);
    out[(size_t)s * D + n] = __float2bfloat16(rbf(g * a) + rbf(rbf(1.f - g) * b));
}

extern "C" void kernel_launch(void* const* d_in, const int* in_sizes, int n_in,
                              void* d_out, int out_size, void* d_ws, size_t ws_size,
                              hipStream_t stream) {
    const bf16* x = (const bf16*)d_in[0];
    const bf16* wqa = (const bf16*)d_in[1];
    const bf16* wpa = (const bf16*)d_in[2];
    const bf16* woa = (const bf16*)d_in[3];
    const bf16* wqb = (const bf16*)d_in[5];
    const bf16* wpb = (const bf16*)d_in[6];
    const bf16* wob = (const bf16*)d_in[7];
    const bf16* gw = (const bf16*)d_in[8];
    float* qkv = (float*)d_ws;
    float* phi = qkv + (size_t)2 * S * N3;
    float* kv = phi + (size_t)4 * S * H * 36;
    float* o = kv + 64 * 684;
    k_qkv<<<dim3(N3 / 64, S / 16, 2), 256, 0, stream>>>(x, wqa, wqb, qkv);
    k_phi<<<(4 * S * H) / 256, 256, 0, stream>>>(qkv, wpa, wpb, phi);
    k_kv<<<64, 704, 0, stream>>>(qkv, phi, kv);
    k_o<<<(2 * S * H) / 256, 256, 0, stream>>>(phi, kv, o);
    k_out<<<S, 576, 0, stream>>>(o, woa, wob, x, gw, (bf16*)d_out);
}
```

```cpp
#include <hip/hip_runtime.h>
#include <hip/hip_bf16.h>
#include <stdint.h>

typedef __bf16 bf8 __attribute__((ext_vector_type(8)));
typedef __bf16 bf4 __attribute__((ext_vector_type(4)));
typedef __bf16 bf2 __attribute__((ext_vector_type(2)));
typedef float f4 __attribute__((ext_vector_type(4)));
typedef float f2 __attribute__((ext_vector_type(2)));
typedef unsigned u4 __attribute__((ext_vector_type(4)));
typedef unsigned u2 __attribute__((ext_vector_type(2)));
#define LAS __attribute__((address_space(3)))
#define DI __device__ __forceinline__

constexpr int XROWB = 1152, WROWB = 3456;
constexpr int L_WT0 = 0;
constexpr int L_WT1 = 6144;
constexpr int L_KVT = 9216;
constexpr int L_GATE = 18816;
constexpr int L_RING = 23424;
constexpr int SLOT = 34816;
constexpr int L_TOTAL = 163840;
constexpr int PHI_P = 304, V_P = 176;
constexpr int L_PHI = L_GATE;
constexpr int L_V = L_RING + SLOT;
constexpr int L_WQ = L_TOTAL - 18 * 4608;
constexpr int L_WO = L_GATE;
constexpr int L_RED = L_WO + 6 * 8192;
static_assert(L_PHI + 128 * PHI_P <= L_V && L_V + 128 * V_P <= L_WQ && L_RED + 8 * 43 * 16 <= L_WQ && L_WO + 17 * 8192 <= L_TOTAL, "lds");
static_assert(L_WQ + 11 * 4608 >= L_RING + 3 * SLOT && L_WQ + 3 * 4608 >= L_RING + 2 * SLOT, "wq slabs");
constexpr int slot_of(int c) { return L_RING + ((8 - c) % 3) * SLOT; }
constexpr int wo_addr(int u) { return u < 6 ? L_WO + u * 8192 : (u < 12 ? L_WO + (u + 5) * 8192 : (u < 17 ? L_WO + (u - 6) * 8192 : 0)); }
constexpr size_t WS_EPOCH = 0;
constexpr size_t WS_FLAG = 4096;
constexpr size_t WS_P1 = 16384;
constexpr int P1_UNIT = 2 * 684 * 16;
constexpr size_t WS_P2 = WS_P1 + (size_t)256 * P1_UNIT;
constexpr int P2_UNIT = 684 * 16;
constexpr size_t WS_O = (WS_P2 + (size_t)16 * P2_UNIT + 4095) / 4096 * 4096;
constexpr size_t WS_OL = WS_O + (size_t)128 * 32 * 1024;
constexpr size_t WS_DBG = WS_OL + (size_t)128 * 32 * 128;
constexpr unsigned SPIN_CAP = 1u << 16;
#define HOP1_ISSUE 10
#define HOP1_CHECK 13
static_assert(L_WO + 11 * 8192 >= L_WQ + (18 - HOP1_CHECK) * 4608, "wo wave B needs these Wq slabs consumed");

DI const void* uni(const void* p) {
    uint64_t v = (uint64_t)p;
    unsigned lo = __builtin_amdgcn_readfirstlane((unsigned)v), hi = __builtin_amdgcn_readfirstlane((unsigned)(v >> 32));
    return (const void*)(((uint64_t)hi << 32) | lo);
}
template <int IMM> DI void dma16(unsigned voff, const void* sbase, unsigned ldsbase) {
    asm volatile("s_add_u32 m0, %2, %3\n\ts_nop 0\n\tglobal_load_lds_dwordx4 %0, %1"
                 :: "v"(voff), "s"(sbase), "s"(ldsbase), "n"(IMM) : "memory", "scc");
}
template <int IMM> DI void dma16m(unsigned voff, const void* sbase, unsigned ldsbase, uint64_t mask) {
    asm volatile("s_add_u32 m0, %2, %3\n\ts_mov_b64 exec, %4\n\tglobal_load_lds_dwordx4 %0, %1\n\ts_mov_b64 exec, -1"
                 :: "v"(voff), "s"(sbase), "s"(ldsbase), "n"(IMM), "s"(mask) : "memory", "scc");
}
DI u4 gld16_sc1(unsigned voff, const void* sbase) { u4 r; asm volatile("s_nop 4\n\tglobal_load_dwordx4 %0, %1, %2 sc1" : "=v"(r) : "v"(voff), "s"(uni(sbase)) : "memory"); return r; }
#define OMOD " sc1"
template <int OFF> DI u4 gld16_sc1o(unsigned voff, const void* sbase) { u4 r; asm volatile("global_load_dwordx4 %0, %1, %2 offset:%3" OMOD : "=v"(r) : "v"(voff), "s"(sbase), "n"(OFF) : "memory"); return r; }
DI u4 gld16_sc1n(unsigned voff, const void* sbase) { u4 r; asm volatile("global_load_dwordx4 %0, %1, %2 sc1" : "=v"(r) : "v"(voff), "s"(sbase) : "memory"); return r; }
template <int OFF> DI u2 gld8_sc1o(unsigned voff, const void* sbase) { u2 r; asm volatile("global_load_dwordx2 %0, %1, %2 offset:%3 sc1" : "=v"(r) : "v"(voff), "s"(sbase), "n"(OFF) : "memory"); return r; }
DI void gst8_sc1(unsigned voff, void* sbase, u2 v) { asm volatile("s_nop 4\n\tglobal_store_dwordx2 %0, %1, %2 sc1" :: "v"(voff), "v"(v), "s"(uni(sbase)) : "memory"); }
DI unsigned gld4_sc1(unsigned voff, const void* sbase) { unsigned r; asm volatile("s_nop 4\n\tglobal_load_dword %0, %1, %2 sc1" : "=v"(r) : "v"(voff), "s"(uni(sbase)) : "memory"); return r; }
DI unsigned gldu16(unsigned voff, const void* sbase) { unsigned r; asm volatile("s_nop 4\n\tglobal_load_ushort %0, %1, %2" : "=v"(r) : "v"(voff), "s"(uni(sbase)) : "memory"); return r; }
DI void gst16_sc1(unsigned voff, void* sbase, u4 v) { asm volatile("s_nop 4\n\tglobal_store_dwordx4 %0, %1, %2 sc1\n\ts_nop 1" :: "v"(voff), "v"(v), "s"(uni(sbase)) : "memory"); }
DI void gst4_sc1(unsigned voff, void* sbase, unsigned v) { asm volatile("s_nop 4\n\tglobal_store_dword %0, %1, %2 sc1" :: "v"(voff), "v"(v), "s"(uni(sbase)) : "memory"); }
#define BAR() do { asm volatile("s_waitcnt lgkmcnt(0)" ::: "memory"); __builtin_amdgcn_s_barrier(); asm volatile("" ::: "memory"); } while (0)
template <int N> DI void waitv() { asm volatile("s_waitcnt vmcnt(%0)" :: "n"(N) : "memory"); }
#define SB() __builtin_amdgcn_sched_barrier(0)
template <int V> struct IC { static constexpr int value = V; };
template <int I, int N, class F> DI void sfor(F&& f) { if constexpr (I < N) { f(IC<I>{}); sfor<I + 1, N>(f); } }

DI bf4 trd(unsigned a) { return __builtin_amdgcn_ds_read_tr16_b64_v4bf16((LAS bf4*)(uintptr_t)a); }
DI bf8 trfrag(unsigned a, int toff) { bf4 lo = trd(a), hi = trd(a + toff); return __builtin_shufflevector(lo, hi, 0, 1, 2, 3, 4, 5, 6, 7); }
DI bf8 ld128(unsigned a) { return *(LAS bf8*)(uintptr_t)a; }
DI bf4 ld64(unsigned a) { return *(LAS bf4*)(uintptr_t)a; }
DI f4 mfma(bf8 a, bf8 b, f4 c) { return __builtin_amdgcn_mfma_f32_16x16x32_bf16(a, b, c, 0, 0, 0); }
DI f4 mfma16(bf4 a, bf4 b, f4 c) { const bf4 z = __builtin_bit_cast(bf4, (u2){0u, 0u}); return mfma(__builtin_shufflevector(a, z, 0, 1, 2, 3, 4, 5, 6, 7), __builtin_shufflevector(b, z, 0, 1, 2, 3, 4, 5, 6, 7), c); }
DI bf4 pack4(f4 v) { return __builtin_convertvector(v, bf4); }
DI bf8 pack8(f4 a, f4 b) { return __builtin_shufflevector(pack4(a), pack4(b), 0, 1, 2, 3, 4, 5, 6, 7); }
DI unsigned pack2(float a, float b) { f2 v = {a, b}; return __builtin_bit_cast(unsigned, __builtin_convertvector(v, bf2)); }
DI unsigned pack2v(f2 v) { return __builtin_bit_cast(unsigned, __builtin_convertvector(v, bf2)); }
DI f2 swap16(float x) { auto r = __builtin_amdgcn_permlane16_swap(__float_as_uint(x), __float_as_uint(x), false, false); return (f2){__uint_as_float(r[0]), __uint_as_float(r[1])}; }

#define STAMP(i) do {} while (0)
#define STAMP_DECL
#define STAMP_OUT(n)

DI void dual_softmax(f4 p0, f4 p1, float p2, unsigned* ph) {
    float mx = fmaxf(fmaxf(p0[0], p0[1]), p2), mn = fminf(fminf(p0[0], p0[1]), p2);
    mx = fmaxf(fmaxf(mx, p0[2]), p0[3]); mn = fminf(fminf(mn, p0[2]), p0[3]);
    mx = fmaxf(fmaxf(mx, p1[0]), p1[1]); mn = fminf(fminf(mn, p1[0]), p1[1]);
    mx = fmaxf(fmaxf(mx, p1[2]), p1[3]); mn = fminf(fminf(mn, p1[2]), p1[3]);
    { f2 t = swap16(mx); mx = fmaxf(t[0], t[1]); }
    { f2 t = swap16(mn); mn = fminf(t[0], t[1]); }
    const float L2E = 1.4426950408889634f;
    const float a = -mx * L2E, b = mn * L2E;
    f2 x[4] = {{p0[0], p0[1]}, {p0[2], p0[3]}, {p1[0], p1[1]}, {p1[2], p1[3]}};
    f2 ep[4], en[4];
#pragma unroll
    for (int i = 0; i < 4; ++i) {
        f2 tp = x[i] * (f2){L2E, L2E} + (f2){a, a};
        f2 tn = x[i] * (f2){-L2E, -L2E} + (f2){b, b};
        ep[i] = (f2){__builtin_amdgcn_exp2f(tp[0]), __builtin_amdgcn_exp2f(tp[1])};
        en[i] = (f2){__builtin_amdgcn_exp2f(tn[0]), __builtin_amdgcn_exp2f(tn[1])};
    }
    f2 t8 = (f2){p2, p2} * (f2){L2E, -L2E} + (f2){a, b};
    f2 e8 = {__builtin_amdgcn_exp2f(t8[0]), __builtin_amdgcn_exp2f(t8[1])};
    f2 sp = (ep[0] + ep[1]) + (ep[2] + ep[3]), sn = (en[0] + en[1]) + (en[2] + en[3]);
    float s1 = (sp[0] + sp[1]) + e8[0], s2 = (sn[0] + sn[1]) + e8[1];
    { f2 t = swap16(s1); s1 = t[0] + t[1]; }
    { f2 t = swap16(s2); s2 = t[0] + t[1]; }
    const float rp = __builtin_amdgcn_rcpf(s1), rn = __builtin_amdgcn_rcpf(s2);
#pragma unroll
    for (int i = 0; i < 4; ++i) { ph[i] = pack2v(ep[i] * (f2){rp, rp}); ph[4 + i] = pack2v(en[i] * (f2){rn, rn}); }
    ph[8] = pack2v(e8 * (f2){rp, rn});
}

__global__ __launch_bounds__(512, 2) void kf(const char* __restrict__ x, const char* __restrict__ wqa, const char* __restrict__ wqb,
                                             const char* __restrict__ wpa, const char* __restrict__ wpb, const char* __restrict__ gw,
                                             const char* __restrict__ woa, const char* __restrict__ wob, char* ws, char* __restrict__ out) {
    __shared__ __attribute__((aligned(16))) char lds[L_TOTAL];
    const int tid = threadIdx.x, lane = tid & 63, w = __builtin_amdgcn_readfirstlane(tid >> 6);
    const int b = blockIdx.x, xcd = b & 7, bi = b >> 3;
    const int rt = (xcd & 1) * 8 + (bi & 7), cg = (xcd >> 1) * 4 + (bi >> 3);
    const int br = cg >> 3, hg = cg & 7;
    const int m = w & 3, hp = w >> 2;
    const int c = lane & 15, g = lane >> 4, q = c >> 2, p = lane & 3;
    const unsigned LB = (unsigned)(uintptr_t)(LAS char*)lds;
    const char* wq = br ? wqb : wqa;
    const char* wp = (br ? wpb : wpa) + hg * 4 * 324 * 2;
    const uint64_t M36 = (1ull << 36) - 1;
    const int rot = ((bi & 7) + 2 * (bi >> 3)) % 9;
    auto phys = [&](int ch) { const int t = ch + rot; return t >= 9 ? t - 9 : t; };
    STAMP_DECL;
    STAMP(0);

    const unsigned vx = (unsigned)((w * 8 + (lane >> 3)) * XROWB + (((lane & 7) ^ ((4 * (w & 1) + (lane >> 4)) & 7)) * 16));
    unsigned vw, vq;
    const int l36 = lane < 36 ? lane : 0;
    const int lr = l36 >= 18, lci = l36 - 18 * lr, lsec = lci >= 9, lcc = lci - 9 * lsec;
    const int pq = 4 * w + 2 * lr + lsec;
    const int kq = 16 * (pq >> 4) + 8 * ((pq >> 3) & 1) + 4 * (pq & 1) + ((pq >> 1) & 3);
    {
        const int pos = 2 * w + lr, t = pos >> 3, j = pos & 7;
        vw = (unsigned)((8 * (j >> 2) + 4 * t + (j & 3)) * WROWB + (576 + 576 * lsec) * 2 + hg * 144 + lcc * 16);
        vq = (unsigned)(kq * WROWB + hg * 144 + lcc * 16);
    }
    const char* xb = x + (size_t)rt * 128 * XROWB;
    const unsigned lw1024 = LB + w * 1024, lw576 = LB + w * 576;
    auto issue_piece = [&](auto C_, auto I_) {
        constexpr int ch = decltype(C_)::value, i = decltype(I_)::value, slot = slot_of(ch);
        const int pc = phys(ch);
        if constexpr (i < 2) dma16<slot + 8 * i * 1024>(vx, xb + pc * 128 + i * 64 * XROWB, lw1024);
        else dma16m<slot + 16384 + (i - 2) * 4608>(vw, wq + (size_t)(pc * 64 + (i - 2) * 16) * WROWB, lw576, M36);
    };
    auto issue_wq = [&](auto S_) {
        constexpr int s = decltype(S_)::value;
        dma16m<L_WQ + (17 - s) * 4608>(vq, wq + (size_t)(phys(s >> 1) * 64 + (s & 1) * 32) * WROWB, lw576, M36);
    };
    sfor<0, 6>([&](auto I_) { issue_piece(IC<0>{}, I_); });
    unsigned gw0 = gldu16(tid * 2, gw);
    unsigned gw1 = gldu16((tid < 64 ? 512 + tid : tid) * 2, gw);
    unsigned wp0 = gldu16(tid * 2, wp), wp1 = gldu16((tid + 512) * 2, wp), wp2 = gldu16((tid < 272 ? tid + 1024 : tid) * 2, wp);
    unsigned ep0 = gld4_sc1(0, ws + WS_EPOCH);
    sfor<0, 6>([&](auto I_) { issue_piece(IC<1>{}, I_); });

    {
        const u4 z = {0, 0, 0, 0};
        *(LAS u4*)(uintptr_t)(LB + tid * 16) = z;
        *(LAS u4*)(uintptr_t)(LB + (tid + 512) * 16) = z;
        if (tid < 152) *(LAS u4*)(uintptr_t)(LB + (tid + 1024) * 16) = z;
    }
    const int jrow = 4 * (g & 1) + q, hrow = g >> 1;
    const unsigned ax0 = (unsigned)((32 * m + c) * 128 + ((g ^ (c >> 1)) * 16));
    const unsigned aw0 = (unsigned)(16384 + (16 * hrow + jrow) * 288 + 72 * hp + 8 * p);
    const bool isgate = p >= 2;
    const unsigned a4base = isgate ? (unsigned)(LB + L_GATE + (hrow * 8 + jrow) * 8)
                                   : (unsigned)(LB + 16384 + (16 * hrow + jrow) * 288 + 72 * hp + 64 + 144 * p);
    waitv<6>();
    asm volatile("" : "+v"(gw0), "+v"(gw1), "+v"(wp0), "+v"(wp1), "+v"(wp2), "+v"(ep0));
    const unsigned tag = __builtin_amdgcn_readfirstlane(ep0) + 1u;
    {
        auto gaddr = [&](int k) {
            int pcx = (k >> 6) - rot; pcx = pcx < 0 ? pcx + 9 : pcx;
            const int s = 2 * pcx + ((k >> 5) & 1), r = k & 31, h = r >> 4, j = ((r >> 3) & 1) * 4 + (r & 3), t = (r >> 2) & 1;
            return LB + L_GATE + t * 2304 + (s * 16 + h * 8 + j) * 8; };
        *(LAS u2*)(uintptr_t)gaddr(tid) = (u2){gw0, 0u};
        if (tid < 64) *(LAS u2*)(uintptr_t)gaddr(512 + tid) = (u2){gw1, 0u};
    }
    BAR();
    STAMP(1);

    bf8 xf[2][18];
    bf8 wf[2][5];
    f4 acc[5][2];
#pragma unroll
    for (int T = 0; T < 5; ++T) { acc[T][0] = (f4){0, 0, 0, 0}; acc[T][1] = (f4){0, 0, 0, 0}; }
    unsigned ax, aw;
    auto reads = [&](auto S_, auto T_) {
        constexpr int s = decltype(S_)::value, T = decltype(T_)::value, ch = s >> 1, ks = s & 1;
        if constexpr (T == 0) {
            ax = LB + slot_of(ch) + (ax0 ^ (ks * 64)); aw = LB + slot_of(ch) + aw0 + ks * 9216;
            asm volatile("" : "+v"(ax), "+v"(aw));
            wf[ks][0] = trfrag(aw, 2304); xf[0][s] = ld128(ax);
        } else if constexpr (T == 1) { xf[1][s] = ld128(ax + 2048); wf[ks][1] = trfrag(aw + 32, 2304); }
        else if constexpr (T == 2) wf[ks][2] = trfrag(aw + 144, 2304);
        else if constexpr (T == 3) wf[ks][3] = trfrag(aw + 176, 2304);
        else {
            const unsigned a4 = a4base + (isgate ? (unsigned)(s * 128) : (unsigned)(slot_of(ch) + ks * 9216));
            wf[ks][4] = trfrag(a4, 2304);
        }
    };
    sfor<0, 5>([&](auto T_) { reads(IC<0>{}, T_); });
    sfor<0, 3>([&](auto I_) { issue_piece(IC<2>{}, I_); });
    {
        auto waddr = [&](int i) {
            int hh = i / 324, rem = i - hh * 324, d = rem / 18, f = rem - d * 18;
            int e = hh & 1, dd = 18 * e + d, hf = f >= 9, f9 = f - 9 * hf, u = f9 >> 2, mm = 8 * e + 4 * hf + (f9 & 3);
            int fr = (hh >> 1) * 3 + u;
            return dd < 32 ? (unsigned)(LB + L_WT0 + (fr * 64 + ((dd & 15) >> 2) * 16 + mm) * 16 + 2 * ((dd & 3) + 4 * (dd >> 4)))
                           : (unsigned)(LB + L_WT1 + (fr * 64 + mm) * 8 + 2 * (dd - 32));
        };
        *(LAS unsigned short*)(uintptr_t)waddr(tid) = (unsigned short)wp0;
        *(LAS unsigned short*)(uintptr_t)waddr(tid + 512) = (unsigned short)wp1;
        if (tid < 272) *(LAS unsigned short*)(uintptr_t)waddr(tid + 1024) = (unsigned short)wp2;
        if (tid < 8) *(LAS unsigned short*)(uintptr_t)(LB + L_WT1 + (((tid >> 2) * 3 + 2) * 64 + 32 + 4 * (tid & 3) + 1) * 8) = (unsigned short)0x3F80;
    }
    sfor<0, 18>([&](auto S_) {
        constexpr int s = decltype(S_)::value, ks = s & 1;
        constexpr int cb = (s + 1) / 2;
        sfor<0, 5>([&](auto T_) {
            constexpr int T = decltype(T_)::value;
            SB();
            acc[T][0] = mfma(wf[ks][T], xf[0][s], acc[T][0]); acc[T][1] = mfma(wf[ks][T], xf[1][s], acc[T][1]);
            SB();
            if constexpr (T == 0 && s + 1 < 18 && (s + 1) % 2 == 0) {
                waitv<(cb <= 7 ? 6 : 4)>();
                BAR();
                if constexpr (cb == 4 || cb == 8) STAMP(cb == 4 ? 10 : 11);
            }
            if constexpr (s + 1 < 18) reads(IC<s + 1>{}, T_);
            if constexpr (T >= 1 && T < 4) {
                constexpr int i = T - 1, cn = cb + 2, half = ks ? 0 : 1;
                if constexpr (cn < 9) issue_piece(IC<cn>{}, IC<3 * half + i>{});
                else if constexpr (i < 2) issue_wq(IC<2 * (s - 13) + i>{});
            }
        });
    });
    SB();
    STAMP(2);
    BAR();

    auto feature_map = [&](const f4* t0, const f4* t1, const f4* t2, f4 (*P)[3]) {
        bf8 A0[3]; bf4 A1[3];
#pragma unroll
        for (int u = 0; u < 3; ++u) { A0[u] = ld128(LB + L_WT0 + ((hp * 3 + u) * 64 + lane) * 16); A1[u] = ld64(LB + L_WT1 + ((hp * 3 + u) * 64 + lane) * 8); }
#pragma unroll
        for (int tt = 0; tt < 2; ++tt) {
            const bf8 B0 = pack8(t0[tt], t1[tt]); const bf4 B1 = pack4(t2[tt]);
#pragma unroll
            for (int u = 0; u < 3; ++u) P[tt][u] = mfma16(A1[u], B1, mfma(A0[u], B0, (f4){0, 0, 0, 0}));
        }
    };
    float glogit[2];
    {
        f4 P[2][3];
        feature_map(acc[0], acc[1], acc[4], P);
        issue_wq(IC<10>{}); issue_wq(IC<11>{});
        const unsigned av = LB + L_V + (m * 32 + c) * V_P + (9 * hp + g) * 8;
        const unsigned av4 = LB + L_V + (m * 32 + c) * V_P + (g == 1 ? (9 * hp + 8) * 8 : 144);
#pragma unroll
        for (int tt = 0; tt < 2; ++tt) {
            *(LAS bf4*)(uintptr_t)(av + tt * 16 * V_P) = pack4(acc[2][tt]);
            *(LAS bf4*)(uintptr_t)(av + tt * 16 * V_P + 32) = pack4(acc[3][tt]);
            u2 val = __builtin_bit_cast(u2, pack4(acc[4][tt]));
            if (g != 1) val = (u2){0x3F80u, 0u};
            *(LAS u2*)(uintptr_t)(av4 + tt * 16 * V_P) = val;
        }
        issue_wq(IC<12>{}); issue_wq(IC<13>{});
        const unsigned ar = LB + L_PHI + (m * 32 + c) * PHI_P + 144 * hp;
        const unsigned am = ar + (g >> 1) * 80 + (g & 1) * 32, at = ar + 64 + 4 * g;
        sfor<0, 2>([&](auto TT_) {
            constexpr int tt = decltype(TT_)::value;
            glogit[tt] = P[tt][2][1];
            unsigned ph[9];
            dual_softmax(P[tt][0], P[tt][1], P[tt][2][0], ph);
            *(LAS u4*)(uintptr_t)(am + tt * 16 * PHI_P) = (u4){ph[0], ph[1], ph[2], ph[3]};
            *(LAS u4*)(uintptr_t)(am + tt * 16 * PHI_P + 16) = (u4){ph[4], ph[5], ph[6], ph[7]};
            *(LAS unsigned*)(uintptr_t)(at + tt * 16 * PHI_P) = ph[8];
            issue_wq(IC<14 + 2 * tt>{}); issue_wq(IC<15 + 2 * tt>{});
        });
    }
    BAR();
    STAMP(3);
    {
        const int hh = w >> 1, fh = w & 1;
        const int rpos = 16 * hrow + 2 * jrow;
        const unsigned aF = LB + L_PHI + rpos * PHI_P + 144 * (hh >> 1) + ((hh & 1) ? 80 : 0) + 32 * fh + 8 * p;
        const unsigned aF2 = LB + L_PHI + rpos * PHI_P + 144 * (p >> 1) + 64 + 8 * (p & 1);
        unsigned aD = LB + L_V + rpos * V_P + (9 * (hh >> 1) + ((hh & 1) ? 5 : 0)) * 8 + 8 * p;
        unsigned aDc = LB + L_V + rpos * V_P + (p == 0 ? 32 : (p == 1 ? 104 : 128 + 8 * p));
        asm volatile("" : "+v"(aD), "+v"(aDc));
        f4 ca = {0, 0, 0, 0}, cb = {0, 0, 0, 0}, cc = {0, 0, 0, 0};
#pragma unroll
        for (int mm = 0; mm < 4; ++mm) {
            bf8 F = trfrag(aF + mm * 32 * PHI_P, PHI_P), F2 = trfrag(aF2 + mm * 32 * PHI_P, PHI_P);
            bf8 Dh = trfrag(aD + mm * 32 * V_P, V_P), Dc = trfrag(aDc + mm * 32 * V_P, V_P);
            ca = mfma(F, Dh, ca); cb = mfma(F, Dc, cb); cc = mfma(F2, fh ? Dc : Dh, cc);
        }
        waitv<0>();
        char* p1 = ws + WS_P1 + (size_t)(cg * 16 + rt) * P1_UNIT;
        auto put = [&](int Q, f4 v) {
            gst16_sc1(Q * 16, p1, (u4){__float_as_uint(v[0]), tag, __float_as_uint(v[1]), tag});
            gst16_sc1(Q * 16 + 684 * 16, p1, (u4){__float_as_uint(v[2]), tag, __float_as_uint(v[3]), tag});
        };
        put(w * 64 + lane, ca);
        const int c0 = 4 * (hh >> 1) + 2 * (hh & 1);
        const int ci = (c == 8) ? 2 : c - c0;
        const bool cval = (c == 8) || (c == c0) || (c == c0 + 1);
        if (cval) put(512 + w * 12 + g * 3 + ci, cb);
        if (g == hh && (fh == 0 || cval)) put(fh == 0 ? 608 + hh * 16 + c : 672 + hh * 3 + ci, cc);
    }
    BAR();
    STAMP(4);
    const int tau = xcd * 30 + bi;
    const bool has2 = bi < 30 && tau < 234;
    const int rg = has2 ? tau / 9 : 0, nt = has2 ? tau - rg * 9 : 0;
    const int rot2 = nt & 7;
    unsigned vm, vl;
    {
        const int j8 = lane >> 3, ch = lane & 7, kk = 16 * ((w >> 1) & 1) + 8 * (j8 >> 2) + 4 * (w & 1) + (j8 & 3);
        const int cb = nt * 128 + (((ch >> 1) ^ ((j8 >> 1) & 3)) * 32) + (ch & 1) * 16;
        vm = (unsigned)((18 * (kk >> 4) + (kk & 15)) * XROWB + cb);
        vl = (unsigned)((18 * (2 * (2 * (kk >> 3) + ((kk & 7) >> 2)) + ((kk >> 1) & 1)) + 16 + (kk & 1)) * XROWB + cb);
    }
    auto issue_wo = [&](auto U_) {
        constexpr int u = decltype(U_)::value, j = u >> 1, kp = u & 1;
        const char* wo = kp ? wob : woa;
        if constexpr (j < 8) dma16<wo_addr(u)>(vm, wo + (size_t)(8 * hp + ((j + rot2) & 7)) * 36 * XROWB, lw1024);
        else dma16<wo_addr(u)>(vl, wo + (size_t)hp * 288 * XROWB, lw1024);
    };
    f4 qa[3][2];
#pragma unroll
    for (int T = 0; T < 3; ++T) { qa[T][0] = (f4){0, 0, 0, 0}; qa[T][1] = (f4){0, 0, 0, 0}; }
    {
        unsigned aq = LB + L_WQ + 9 * 4608 + (hrow * 8 + jrow) * 288 + 72 * hp + 8 * p;
        unsigned aq8 = LB + L_WQ + 9 * 4608 + (hrow * 8 + jrow) * 288 + 72 * hp + 64;
        asm volatile("" : "+v"(aq), "+v"(aq8));
        const int nq = rt == 15 ? 39 : 43;
        const int Q = rt * 43 + (lane < nq ? lane : 0);
        const char* p1 = ws + WS_P1 + (size_t)(cg * 16 + 2 * w) * P1_UNIT;
        u4 a0, a1, b0, b1;
        auto hop1_load = [&]() {
            a0 = gld16_sc1(Q * 16, p1); a1 = gld16_sc1(Q * 16 + 684 * 16, p1);
            b0 = gld16_sc1(Q * 16 + P1_UNIT, p1); b1 = gld16_sc1(Q * 16 + 684 * 16 + P1_UNIT, p1);
        };
        bf8 wqf[2][3];
        auto qreads = [&](auto S_, auto T_) {
            constexpr int s = decltype(S_)::value, T = decltype(T_)::value;
            if constexpr (s == 9 && T == 0) { aq -= 9 * 4608; aq8 -= 9 * 4608; asm volatile("" : "+v"(aq), "+v"(aq8)); }
            constexpr int o = ((17 - s) % 9) * 4608;
            if constexpr (T < 2) wqf[s & 1][T] = trfrag(aq + o + T * 32, 144); else wqf[s & 1][2] = trfrag(aq8 + o, 144);
        };
        sfor<0, 3>([&](auto T_) { qreads(IC<0>{}, T_); });
        sfor<0, 18>([&](auto S_) {
            constexpr int s = decltype(S_)::value;
            if constexpr (s == HOP1_ISSUE) hop1_load();
            if constexpr (s == HOP1_CHECK) {
                STAMP(5);
                asm volatile("s_waitcnt vmcnt(0)" : "+v"(a0), "+v"(a1), "+v"(b0), "+v"(b1) :: "memory");
                for (unsigned spins = 0;; ++spins) {
                    bool ok = a0[1] == tag && a0[3] == tag && a1[1] == tag && a1[3] == tag && b0[1] == tag && b0[3] == tag && b1[1] == tag && b1[3] == tag;
                    if (__all(ok) || spins > SPIN_CAP) break;
                    __builtin_amdgcn_s_sleep(1);
                    hop1_load();
                    asm volatile("s_waitcnt vmcnt(0)" : "+v"(a0), "+v"(a1), "+v"(b0), "+v"(b1) :: "memory");
                }
                f4 sum = {__uint_as_float(a0[0]) + __uint_as_float(b0[0]), __uint_as_float(a0[2]) + __uint_as_float(b0[2]),
                          __uint_as_float(a1[0]) + __uint_as_float(b1[0]), __uint_as_float(a1[2]) + __uint_as_float(b1[2])};
                if (lane < 43) *(LAS f4*)(uintptr_t)(LB + L_RED + (w * 43 + lane) * 16) = sum;
                BAR();
                if (w == 0 && lane < nq) {
                    f4 t = *(LAS f4*)(uintptr_t)(LB + L_RED + lane * 16);
#pragma unroll
                    for (int i = 1; i < 8; ++i) t += *(LAS f4*)(uintptr_t)(LB + L_RED + (i * 43 + lane) * 16);
                    gst16_sc1(Q * 16, ws + WS_P2 + (size_t)cg * P2_UNIT, (u4){pack2(t[0], t[1]), tag, pack2(t[2], t[3]), tag});
                }
                STAMP(6);
            }
            sfor<0, 3>([&](auto T_) {
                constexpr int T = decltype(T_)::value;
                SB();
                qa[T][0] = mfma(wqf[s & 1][T], xf[0][s], qa[T][0]); qa[T][1] = mfma(wqf[s & 1][T], xf[1][s], qa[T][1]);
                SB();
                if constexpr (s + 1 < 18) qreads(IC<s + 1>{}, T_);
                if constexpr (s < 6 && T == 0) issue_wo(IC<s>{});
                else if constexpr (s >= HOP1_CHECK && T == 0) issue_wo(IC<6 + (s - HOP1_CHECK)>{});
            });
        });
    }
    constexpr int WB0 = 6 + 18 - HOP1_CHECK;
    static_assert(WB0 <= 12, "");
    SB();
    STAMP(7);
    BAR();
    unsigned phq[2][9];
    {
        f4 P[2][3];
        feature_map(qa[0], qa[1], qa[2], P);
        sfor<WB0, WB0 + 3>([&](auto G_) { issue_wo(G_); });
        dual_softmax(P[0][0], P[0][1], P[0][2][0], phq[0]);
        SB();
        sfor<WB0 + 3, 17>([&](auto G_) { issue_wo(G_); });
        dual_softmax(P[1][0], P[1][1], P[1][2][0], phq[1]);
        SB();
    }
    STAMP(8);
    {
        const char* p2 = ws + WS_P2 + (size_t)cg * P2_UNIT;
        const int Q0 = tid, Q1 = tid < 172 ? 512 + tid : tid;
        u4 a0, a1;
        for (unsigned spins = 0;; ++spins) {
            a0 = gld16_sc1(Q0 * 16, p2); a1 = gld16_sc1(Q1 * 16, p2);
            asm volatile("s_waitcnt vmcnt(0)" : "+v"(a0), "+v"(a1) :: "memory");
            bool ok = a0[1] == tag && a0[3] == tag && a1[1] == tag && a1[3] == tag;
            if (__all(ok) || spins > SPIN_CAP) break;
            __builtin_amdgcn_s_sleep(1);
        }
        auto putq = [&](int hh, int d, int fq, unsigned lo, unsigned hi) {
            const int r0 = d < 16 ? (d >> 3) * 12 + (d & 7) : d - 8, r1 = d < 16 ? r0 : d + 4;
            const unsigned base = LB + L_KVT + hh * 24 * 80 + fq * 8, step = fq == 8 ? 8 : 4;
            *(LAS unsigned*)(uintptr_t)(base + r0 * 80) = lo; *(LAS unsigned*)(uintptr_t)(base + r0 * 80 + step) = hi;
            *(LAS unsigned*)(uintptr_t)(base + r1 * 80) = lo; *(LAS unsigned*)(uintptr_t)(base + r1 * 80 + step) = hi;
        };
        putq(w >> 1, c + 2 * ((w >> 1) & 1), 4 * (w & 1) + g, a0[0], a0[2]);
        if (tid < 172) {
            int hh, d, fq;
            if (tid < 96) { int ww = tid / 12, r = tid % 12, rgi = r / 3, ci = r % 3; hh = ww >> 1; fq = 4 * (ww & 1) + rgi; d = ci == 2 ? 18 : ((hh & 1) ? ci : 16 + ci); }
            else if (tid < 160) { int r = tid - 96; hh = r >> 4; fq = 8; d = (r & 15) + 2 * (hh & 1); }
            else { int r = tid - 160; hh = r / 3; int ci = r % 3; fq = 8; d = ci == 2 ? 18 : ((hh & 1) ? ci : 16 + ci); }
            putq(hh, d, fq, a1[0], a1[2]);
        }
        BAR();
        issue_wo(IC<17>{});
    }
    STAMP(9);
    {
        const unsigned ak = LB + L_KVT + ((((c >> 3) == (g >> 1)) ? 2 * hp + (g >> 1) : 4) * 24 + ((c >> 2) & 1) * 12 + (c & 3)) * 80;
        bf8 A[3][2]; bf4 At[3];
#pragma unroll
        for (int u = 0; u < 3; ++u) {
            A[u][0] = ld128(ak + (g & 1) * 32 + u * 320); A[u][1] = ld128(ak + (g & 1) * 32 + u * 320 + 16);
            At[u] = ld64(ak + 64 + (g & 1) * 8 + u * 320);
        }
        const int P = br * 16 + hg * 2 + hp;
        char* ob = ws + WS_O + (size_t)(rt * 8 + 2 * m) * 32 * 1024;
        unsigned left[2];
#pragma unroll
        for (int tt = 0; tt < 2; ++tt) {
            const bf8 B0 = __builtin_bit_cast(bf8, (u4){phq[tt][0], phq[tt][1], phq[tt][2], phq[tt][3]});
            const bf8 B1 = __builtin_bit_cast(bf8, (u4){phq[tt][4], phq[tt][5], phq[tt][6], phq[tt][7]});
            const bf4 B2 = __builtin_bit_cast(bf4, (u2){phq[tt][8], 0u});
            f4 nu[3];
#pragma unroll
            for (int u = 0; u < 3; ++u) nu[u] = mfma16(At[u], B2, mfma(A[u][1], B1, mfma(A[u][0], B0, (f4){0, 0, 0, 0})));
            const float gt = __builtin_amdgcn_rcpf(1.f + __builtin_amdgcn_exp2f(-1.4426950408889634f * glogit[tt]));
            const float sc = (br ? 1.f - gt : gt) * __builtin_amdgcn_rcpf(nu[2][2] + 1e-6f);
            gst16_sc1(tt * 32 * 1024 + P * 1024 + lane * 16, ob, __builtin_bit_cast(u4, pack8(nu[0] * sc, nu[1] * sc)));
            left[tt] = pack2(nu[2][0] * sc, nu[2][1] * sc);
        }
        {
            auto r = __builtin_amdgcn_permlane32_swap(left[0], left[1], false, false);
            if (!(g & 1)) gst8_sc1((unsigned)((((rt * 8 + 2 * m + (g >> 1)) * 32 + P) * 16 + c) * 8), ws + WS_OL, (u2){r[0], r[1]});
        }
        waitv<0>();
        if (lane == 0) gst4_sc1(((rt * 4 + m) * 32 + P) * 4, ws + WS_FLAG, tag);
    }
    STAMP(12);
    BAR();
    if (has2) {
        const int kq2 = w & 3, mh = w >> 2;
        const int T0 = rg * 5 + 3 * mh;
        const int Tc0 = T0 < 127 ? T0 : 127, Tc1 = T0 + 1 < 127 ? T0 + 1 : 127, Tc2 = T0 + 2 < 127 ? T0 + 2 : 127;
        {
            const int Tl = mh ? Tc1 : Tc2;
            const unsigned fo = (unsigned)((((lane & 8) ? (Tl >> 1) : (Tc0 >> 1)) * 32 + 8 * kq2 + (lane & 7)) * 4);
            for (unsigned spins = 0;; ++spins) {
                unsigned f = gld4_sc1(fo, ws + WS_FLAG);
                asm volatile("s_waitcnt vmcnt(0)" : "+v"(f) :: "memory");
                if (__all(f == tag) || spins > SPIN_CAP) break;
                __builtin_amdgcn_s_sleep(1);
            }
        }
        STAMP(13);
        const char* ob = ws + WS_O + (size_t)kq2 * 8 * 1024;
        const char* ol = ws + WS_OL + (size_t)kq2 * 8 * 128;
        const unsigned vo[3] = {(unsigned)(Tc0 * 32768 + lane * 16), (unsigned)(Tc1 * 32768 + lane * 16), (unsigned)(Tc2 * 32768 + lane * 16)};
        const unsigned vol[3] = {(unsigned)(Tc0 * 4096 + g * 256 + c * 8), (unsigned)(Tc1 * 4096 + g * 256 + c * 8), (unsigned)(Tc2 * 4096 + g * 256 + c * 8)};
        unsigned au[4];
#pragma unroll
        for (int u = 0; u < 4; ++u) au[u] = LB + kq2 * 4096 + hrow * 2048 + jrow * 128 + ((((u + kq2) & 3) ^ (jrow >> 1)) * 32) + 8 * p;
        const int last_off = kq2 < 2 ? wo_addr(16) : wo_addr(17) - 8192;
        f4 oc[3][4];
        auto gemm2 = [&](auto NT_) {
            constexpr int NTL = decltype(NT_)::value;
            u4 of[8][NTL]; u2 ofl[NTL][2];
            sfor<0, 8>([&](auto J_) {
                constexpr int j = decltype(J_)::value;
                const char* src = ob + ((j + rot2) & 7) * 1024;
#pragma unroll
                for (int t = 0; t < NTL; ++t) of[j][t] = gld16_sc1n(vo[t], src);
            });
#pragma unroll
            for (int t = 0; t < NTL; ++t) { ofl[t][0] = gld8_sc1o<0>(vol[t], ol); ofl[t][1] = gld8_sc1o<128>(vol[t], ol); }
#pragma unroll
            for (int t = 0; t < NTL; ++t)
#pragma unroll
                for (int u = 0; u < 4; ++u) oc[t][u] = (f4){0, 0, 0, 0};
            bf8 wn[2][4];
            auto oreads = [&](auto J_) {
                constexpr int j = decltype(J_)::value;
#pragma unroll
                for (int u = 0; u < 4; ++u) { unsigned a = au[u] + (j < 8 ? wo_addr(2 * j) : last_off); asm volatile("" : "+v"(a)); wn[j & 1][u] = trfrag(a, 1024); }
            };
            oreads(IC<0>{});
            sfor<0, 9>([&](auto J_) {
                constexpr int j = decltype(J_)::value;
                SB();
                if constexpr (j + 1 < 9) oreads(IC<j + 1>{});
                sfor<0, NTL>([&](auto T_) {
                    constexpr int t = decltype(T_)::value;
                    bf8 B;
                    if constexpr (j < 8) {
                        asm volatile("s_waitcnt vmcnt(%1)" : "+v"(of[j][t]) : "n"((7 - j) * NTL + (NTL - 1 - t) + 2 * NTL) : "memory");
                        B = __builtin_bit_cast(bf8, of[j][t]);
                    } else {
                        asm volatile("s_waitcnt vmcnt(%2)" : "+v"(ofl[t][0]), "+v"(ofl[t][1]) : "n"(2 * (NTL - 1 - t)) : "memory");
                        B = __builtin_bit_cast(bf8, (u4){ofl[t][0][0], ofl[t][0][1], ofl[t][1][0], ofl[t][1][1]});
                    }
#pragma unroll
                    for (int u = 0; u < 4; ++u) oc[t][u] = mfma(wn[j & 1][u], B, oc[t][u]);
                });
            });
            SB();
        };
        if (mh == 0) gemm2(IC<3>{});
        else {
            gemm2(IC<2>{});
#pragma unroll
            for (int u = 0; u < 4; ++u) oc[2][u] = (f4){0, 0, 0, 0};
        }
        STAMP(14);
        BAR();
        const int ntl = 3 - mh;
#pragma unroll
        for (int t = 0; t < 3; ++t)
#pragma unroll
            for (int u = 1; u < 4; ++u)
                if (t < ntl) *(LAS f4*)(uintptr_t)(LB + (((kq2 * 5 + 3 * mh + t) * 4 + ((u + kq2) & 3)) * 64 + lane) * 16) = oc[t][u];
        BAR();
#pragma unroll
        for (int t = 0; t < 3; ++t) {
            if (t < ntl && T0 + t < 128) {
                f4 v = oc[t][0];
#pragma unroll
                for (int sq = 1; sq < 4; ++sq) v += *(LAS f4*)(uintptr_t)(LB + (((((sq + kq2) & 3) * 5 + 3 * mh + t) * 4 + kq2) * 64 + lane) * 16);
                *(bf4*)(out + (size_t)((T0 + t) * 16 + c) * XROWB + nt * 128 + kq2 * 32 + g * 8) = pack4(v);
            }
        }
        if (b == 0 && tid == 0) gst4_sc1(0, ws + WS_EPOCH, tag);
    }
    STAMP(15);
    STAMP_OUT(NSTAMPS);
}

extern "C" void kernel_launch(void* const* d_in, const int* in_sizes, int n_in,
                              void* d_out, int out_size, void* d_ws, size_t ws_size,
                              hipStream_t stream) {
    kf<<<256, 512, 0, stream>>>((const char*)d_in[0], (const char*)d_in[1], (const char*)d_in[5], (const char*)d_in[2], (const char*)d_in[6],
                                (const char*)d_in[8], (const char*)d_in[3], (const char*)d_in[7], (char*)d_ws, (char*)d_out);
}
```

```cpp
#include <hip/hip_runtime.h>
#include <hip/hip_bf16.h>
#include <stdint.h>

typedef __bf16 bf8 __attribute__((ext_vector_type(8)));
typedef __bf16 bf4 __attribute__((ext_vector_type(4)));
typedef __bf16 bf2 __attribute__((ext_vector_type(2)));
typedef float f4 __attribute__((ext_vector_type(4)));
typedef float f2 __attribute__((ext_vector_type(2)));
typedef unsigned u4 __attribute__((ext_vector_type(4)));
typedef unsigned u2 __attribute__((ext_vector_type(2)));
#define LAS __attribute__((address_space(3)))
#define DI __device__ __forceinline__

constexpr int XROWB = 1152, WROWB = 3456;
constexpr int L_WT0 = 0;
constexpr int L_WT1 = 6144;
constexpr int L_KVT = 9216;
constexpr int L_GATE = 18816;
constexpr int L_RING = 23424;
constexpr int SLOT = 34816;
constexpr int L_TOTAL = 163840;
constexpr int PHI_P = 304, V_P = 176;
constexpr int L_PHI = L_GATE;
constexpr int L_V = L_RING + SLOT;
constexpr int L_WQ = L_TOTAL - 18 * 4608;
constexpr int L_WO = L_GATE;
constexpr int L_RED = L_WO + 6 * 8192;
static_assert(L_PHI + 128 * PHI_P <= L_V && L_V + 128 * V_P <= L_WQ && L_RED + 8 * 43 * 16 <= L_WQ && L_WO + 17 * 8192 <= L_TOTAL, "lds");
static_assert(L_WQ + 11 * 4608 >= L_RING + 3 * SLOT && L_WQ + 3 * 4608 >= L_RING + 2 * SLOT, "wq slabs");
constexpr int slot_of(int c) { return L_RING + ((8 - c) % 3) * SLOT; }
constexpr int wo_addr(int u) { return u < 6 ? L_WO + u * 8192 : (u < 12 ? L_WO + (u + 5) * 8192 : (u < 17 ? L_WO + (u - 6) * 8192 : 0)); }
constexpr size_t WS_EPOCH = 0;
constexpr size_t WS_FLAG = 4096;
constexpr size_t WS_P1 = 16384;
constexpr int P1_UNIT = 2 * 684 * 16;
constexpr size_t WS_P2 = WS_P1 + (size_t)256 * P1_UNIT;
constexpr int P2_UNIT = 684 * 16;
constexpr size_t WS_O = (WS_P2 + (size_t)16 * P2_UNIT + 4095) / 4096 * 4096;
constexpr size_t WS_OL = WS_O + (size_t)128 * 32 * 1024;
constexpr size_t WS_DBG = WS_OL + (size_t)128 * 32 * 128;
constexpr unsigned SPIN_CAP = 1u << 16;
#define HOP1_ISSUE 10
#define HOP1_CHECK 13
static_assert(L_WO + 11 * 8192 >= L_WQ + (18 - HOP1_CHECK) * 4608, "wo wave B needs these Wq slabs consumed");

DI const void* uni(const void* p) {
    uint64_t v = (uint64_t)p;
    unsigned lo = __builtin_amdgcn_readfirstlane((unsigned)v), hi = __builtin_amdgcn_readfirstlane((unsigned)(v >> 32));
    return (const void*)(((uint64_t)hi << 32) | lo);
}
template <int IMM> DI void dma16(unsigned voff, const void* sbase, unsigned ldsbase) {
    asm volatile("s_add_u32 m0, %2, %3\n\ts_nop 0\n\tglobal_load_lds_dwordx4 %0, %1"
                 :: "v"(voff), "s"(sbase), "s"(ldsbase), "n"(IMM) : "memory", "scc");
}
template <int IMM> DI void dma16m(unsigned voff, const void* sbase, unsigned ldsbase, uint64_t mask) {
    asm volatile("s_add_u32 m0, %2, %3\n\ts_mov_b64 exec, %4\n\tglobal_load_lds_dwordx4 %0, %1\n\ts_mov_b64 exec, -1"
                 :: "v"(voff), "s"(sbase), "s"(ldsbase), "n"(IMM), "s"(mask) : "memory", "scc");
}
DI u4 gld16_sc1(unsigned voff, const void* sbase) { u4 r; asm volatile("s_nop 4\n\tglobal_load_dwordx4 %0, %1, %2 sc1" : "=v"(r) : "v"(voff), "s"(uni(sbase)) : "memory"); return r; }
#define OMOD " sc1"
template <int OFF> DI u4 gld16_sc1o(unsigned voff, const void* sbase) { u4 r; asm volatile("global_load_dwordx4 %0, %1, %2 offset:%3" OMOD : "=v"(r) : "v"(voff), "s"(sbase), "n"(OFF) : "memory"); return r; }
DI u4 gld16_sc1n(unsigned voff, const void* sbase) { u4 r; asm volatile("global_load_dwordx4 %0, %1, %2 sc1" : "=v"(r) : "v"(voff), "s"(sbase) : "memory"); return r; }
template <int OFF> DI u2 gld8_sc1o(unsigned voff, const void* sbase) { u2 r; asm volatile("global_load_dwordx2 %0, %1, %2 offset:%3 sc1" : "=v"(r) : "v"(voff), "s"(sbase), "n"(OFF) : "memory"); return r; }
DI void gst8_sc1(unsigned voff, void* sbase, u2 v) { asm volatile("s_nop 4\n\tglobal_store_dwordx2 %0, %1, %2 sc1" :: "v"(voff), "v"(v), "s"(uni(sbase)) : "memory"); }
DI unsigned gld4_sc1(unsigned voff, const void* sbase) { unsigned r; asm volatile("s_nop 4\n\tglobal_load_dword %0, %1, %2 sc1" : "=v"(r) : "v"(voff), "s"(uni(sbase)) : "memory"); return r; }
DI unsigned gldu16(unsigned voff, const void* sbase) { unsigned r; asm volatile("s_nop 4\n\tglobal_load_ushort %0, %1, %2" : "=v"(r) : "v"(voff), "s"(uni(sbase)) : "memory"); return r; }
DI void gst16_sc1(unsigned voff, void* sbase, u4 v) { asm volatile("s_nop 4\n\tglobal_store_dwordx4 %0, %1, %2 sc1\n\ts_nop 1" :: "v"(voff), "v"(v), "s"(uni(sbase)) : "memory"); }
DI void gst4_sc1(unsigned voff, void* sbase, unsigned v) { asm volatile("s_nop 4\n\tglobal_store_dword %0, %1, %2 sc1" :: "v"(voff), "v"(v), "s"(uni(sbase)) : "memory"); }
#define BAR() do { asm volatile("s_waitcnt lgkmcnt(0)" ::: "memory"); __builtin_amdgcn_s_barrier(); asm volatile("" ::: "memory"); } while (0)
template <int N> DI void waitv() { asm volatile("s_waitcnt vmcnt(%0)" :: "n"(N) : "memory"); }
#define SB() __builtin_amdgcn_sched_barrier(0)
template <int V> struct IC { static constexpr int value = V; };
template <int I, int N, class F> DI void sfor(F&& f) { if constexpr (I < N) { f(IC<I>{}); sfor<I + 1, N>(f); } }

DI bf4 trd(unsigned a) { return __builtin_amdgcn_ds_read_tr16_b64_v4bf16((LAS bf4*)(uintptr_t)a); }
DI bf8 trfrag(unsigned a, int toff) { bf4 lo = trd(a), hi = trd(a + toff); return __builtin_shufflevector(lo, hi, 0, 1, 2, 3, 4, 5, 6, 7); }
DI bf8 ld128(unsigned a) { return *(LAS bf8*)(uintptr_t)a; }
DI bf4 ld64(unsigned a) { return *(LAS bf4*)(uintptr_t)a; }
DI f4 mfma(bf8 a, bf8 b, f4 c) { return __builtin_amdgcn_mfma_f32_16x16x32_bf16(a, b, c, 0, 0, 0); }
DI f4 mfma16(bf4 a, bf4 b, f4 c) { const bf4 z = __builtin_bit_cast(bf4, (u2){0u, 0u}); return mfma(__builtin_shufflevector(a, z, 0, 1, 2, 3, 4, 5, 6, 7), __builtin_shufflevector(b, z, 0, 1, 2, 3, 4, 5, 6, 7), c); }
DI bf4 pack4(f4 v) { return __builtin_convertvector(v, bf4); }
DI bf8 pack8(f4 a, f4 b) { return __builtin_shufflevector(pack4(a), pack4(b), 0, 1, 2, 3, 4, 5, 6, 7); }
DI unsigned pack2(float a, float b) { f2 v = {a, b}; return __builtin_bit_cast(unsigned, __builtin_convertvector(v, bf2)); }
DI unsigned pack2v(f2 v) { return __builtin_bit_cast(unsigned, __builtin_convertvector(v, bf2)); }
DI f2 swap16(float x) { auto r = __builtin_amdgcn_permlane16_swap(__float_as_uint(x), __float_as_uint(x), false, false); return (f2){__uint_as_float(r[0]), __uint_as_float(r[1])}; }

#define STAMP(i) do {} while (0)
#define STAMP_DECL
#define STAMP_OUT(n)

DI void dual_softmax(f4 p0, f4 p1, float p2, unsigned* ph) {
    float mx = fmaxf(fmaxf(p0[0], p0[1]), p2), mn = fminf(fminf(p0[0], p0[1]), p2);
    mx = fmaxf(fmaxf(mx, p0[2]), p0[3]); mn = fminf(fminf(mn, p0[2]), p0[3]);
    mx = fmaxf(fmaxf(mx, p1[0]), p1[1]); mn = fminf(fminf(mn, p1[0]), p1[1]);
    mx = fmaxf(fmaxf(mx, p1[2]), p1[3]); mn = fminf(fminf(mn, p1[2]), p1[3]);
    { f2 t = swap16(mx); mx = fmaxf(t[0], t[1]); }
    { f2 t = swap16(mn); mn = fminf(t[0], t[1]); }
    const float L2E = 1.4426950408889634f;
    const float a = -mx * L2E, b = mn * L2E;
    f2 x[4] = {{p0[0], p0[1]}, {p0[2], p0[3]}, {p1[0], p1[1]}, {p1[2], p1[3]}};
    f2 ep[4], en[4];
#pragma unroll
    for (int i = 0; i < 4; ++i) {
        f2 tp = x[i] * (f2){L2E, L2E} + (f2){a, a};
        f2 tn = x[i] * (f2){-L2E, -L2E} + (f2){b, b};
        ep[i] = (f2){__builtin_amdgcn_exp2f(tp[0]), __builtin_amdgcn_exp2f(tp[1])};
        en[i] = (f2){__builtin_amdgcn_exp2f(tn[0]), __builtin_amdgcn_exp2f(tn[1])};
    }
    f2 t8 = (f2){p2, p2} * (f2){L2E, -L2E} + (f2){a, b};
    f2 e8 = {__builtin_amdgcn_exp2f(t8[0]), __builtin_amdgcn_exp2f(t8[1])};
    f2 sp = (ep[0] + ep[1]) + (ep[2] + ep[3]), sn = (en[0] + en[1]) + (en[2] + en[3]);
    float s1 = (sp[0] + sp[1]) + e8[0], s2 = (sn[0] + sn[1]) + e8[1];
    { f2 t = swap16(s1); s1 = t[0] + t[1]; }
    { f2 t = swap16(s2); s2 = t[0] + t[1]; }
    const float rp = __builtin_amdgcn_rcpf(s1), rn = __builtin_amdgcn_rcpf(s2);
#pragma unroll
    for (int i = 0; i < 4; ++i) { ph[i] = pack2v(ep[i] * (f2){rp, rp}); ph[4 + i] = pack2v(en[i] * (f2){rn, rn}); }
    ph[8] = pack2v(e8 * (f2){rp, rn});
}

__global__ __launch_bounds__(512, 2) void kf(const char* __restrict__ x, const char* __restrict__ wqa, const char* __restrict__ wqb,
                                             const char* __restrict__ wpa, const char* __restrict__ wpb, const char* __restrict__ gw,
                                             const char* __restrict__ woa, const char* __restrict__ wob, char* ws, char* __restrict__ out) {
    __shared__ __attribute__((aligned(16))) char lds[L_TOTAL];
    const int tid = threadIdx.x, lane = tid & 63, w = __builtin_amdgcn_readfirstlane(tid >> 6);
    const int b = blockIdx.x, xcd = b & 7, bi = b >> 3;
    const int rt = (xcd & 1) * 8 + (bi & 7), cg = (xcd >> 1) * 4 + (bi >> 3);
    const int br = cg >> 3, hg = cg & 7;
    const int m = w & 3, hp = w >> 2;
    const int c = lane & 15, g = lane >> 4, q = c >> 2, p = lane & 3;
    const unsigned LB = (unsigned)(uintptr_t)(LAS char*)lds;
    const char* wq = br ? wqb : wqa;
    const char* wp = (br ? wpb : wpa) + hg * 4 * 324 * 2;
    const uint64_t M36 = (1ull << 36) - 1;
    const int rot = ((bi & 7) + 2 * (bi >> 3)) % 9;
    auto phys = [&](int ch) { const int t = ch + rot; return t >= 9 ? t - 9 : t; };
    STAMP_DECL;
    STAMP(0);

    const unsigned vx = (unsigned)((w * 8 + (lane >> 3)) * XROWB + (((lane & 7) ^ ((4 * (w & 1) + (lane >> 4)) & 7)) * 16));
    unsigned vw, vq;
    const int l36 = lane < 36 ? lane : 0;
    const int lr = l36 >= 18, lci = l36 - 18 * lr, lsec = lci >= 9, lcc = lci - 9 * lsec;
    const int pq = 4 * w + 2 * lr + lsec;
    const int kq = 16 * (pq >> 4) + 8 * ((pq >> 3) & 1) + 4 * (pq & 1) + ((pq >> 1) & 3);
    {
        const int pos = 2 * w + lr, t = pos >> 3, j = pos & 7;
        vw = (unsigned)((8 * (j >> 2) + 4 * t + (j & 3)) * WROWB + (576 + 576 * lsec) * 2 + hg * 144 + lcc * 16);
        vq = (unsigned)(kq * WROWB + hg * 144 + lcc * 16);
    }
    const char* xb = x + (size_t)rt * 128 * XROWB;
    const unsigned lw1024 = LB + w * 1024, lw576 = LB + w * 576;
    auto issue_piece = [&](auto C_, auto I_) {
        constexpr int ch = decltype(C_)::value, i = decltype(I_)::value, slot = slot_of(ch);
        const int pc = phys(ch);
        if constexpr (i < 2) dma16<slot + 8 * i * 1024>(vx, xb + pc * 128 + i * 64 * XROWB, lw1024);
        else dma16m<slot + 16384 + (i - 2) * 4608>(vw, wq + (size_t)(pc * 64 + (i - 2) * 16) * WROWB, lw576, M36);
    };
    auto issue_wq = [&](auto S_) {
        constexpr int s = decltype(S_)::value;
        dma16m<L_WQ + (17 - s) * 4608>(vq, wq + (size_t)(phys(s >> 1) * 64 + (s & 1) * 32) * WROWB, lw576, M36);
    };
    sfor<0, 6>([&](auto I_) { issue_piece(IC<0>{}, I_); });
    unsigned cpf;
    {
        uint64_t pc; unsigned len;
        asm volatile("s_getpc_b64 %0\n\tLkf_here%=:\n\ts_mov_b32 %1, Lkf_end - Lkf_here%=" : "=s"(pc), "=s"(len));
        unsigned off = (unsigned)((bi * 8 + w) * 128 + (lane & 1) * 32768);
        off = off + 4 <= len ? off : 0;
        asm volatile("global_load_dword %0, %1, %2" : "=v"(cpf) : "v"(off), "s"(pc) : "memory");
    }
    unsigned gw0 = gldu16(tid * 2, gw);
    unsigned gw1 = gldu16((tid < 64 ? 512 + tid : tid) * 2, gw);
    unsigned wp0 = gldu16(tid * 2, wp), wp1 = gldu16((tid + 512) * 2, wp), wp2 = gldu16((tid < 272 ? tid + 1024 : tid) * 2, wp);
    unsigned ep0 = gld4_sc1(0, ws + WS_EPOCH);
    sfor<0, 6>([&](auto I_) { issue_piece(IC<1>{}, I_); });

    {
        const u4 z = {0, 0, 0, 0};
        *(LAS u4*)(uintptr_t)(LB + tid * 16) = z;
        *(LAS u4*)(uintptr_t)(LB + (tid + 512) * 16) = z;
        if (tid < 152) *(LAS u4*)(uintptr_t)(LB + (tid + 1024) * 16) = z;
    }
    const int jrow = 4 * (g & 1) + q, hrow = g >> 1;
    const unsigned ax0 = (unsigned)((32 * m + c) * 128 + ((g ^ (c >> 1)) * 16));
    const unsigned aw0 = (unsigned)(16384 + (16 * hrow + jrow) * 288 + 72 * hp + 8 * p);
    const bool isgate = p >= 2;
    const unsigned a4base = isgate ? (unsigned)(LB + L_GATE + (hrow * 8 + jrow) * 8)
                                   : (unsigned)(LB + 16384 + (16 * hrow + jrow) * 288 + 72 * hp + 64 + 144 * p);
    waitv<6>();
    asm volatile("" : "+v"(gw0), "+v"(gw1), "+v"(wp0), "+v"(wp1), "+v"(wp2), "+v"(ep0));
    asm volatile("" :: "v"(cpf));
    const unsigned tag = __builtin_amdgcn_readfirstlane(ep0) + 1u;
    {
        auto gaddr = [&](int k) {
            int pcx = (k >> 6) - rot; pcx = pcx < 0 ? pcx + 9 : pcx;
            const int s = 2 * pcx + ((k >> 5) & 1), r = k & 31, h = r >> 4, j = ((r >> 3) & 1) * 4 + (r & 3), t = (r >> 2) & 1;
            return LB + L_GATE + t * 2304 + (s * 16 + h * 8 + j) * 8; };
        *(LAS u2*)(uintptr_t)gaddr(tid) = (u2){gw0, 0u};
        if (tid < 64) *(LAS u2*)(uintptr_t)gaddr(512 + tid) = (u2){gw1, 0u};
    }
    BAR();
    STAMP(1);

    bf8 xf[2][18];
    bf8 wf[2][5];
    f4 acc[5][2];
#pragma unroll
    for (int T = 0; T < 5; ++T) { acc[T][0] = (f4){0, 0, 0, 0}; acc[T][1] = (f4){0, 0, 0, 0}; }
    unsigned ax, aw;
    auto reads = [&](auto S_, auto T_) {
        constexpr int s = decltype(S_)::value, T = decltype(T_)::value, ch = s >> 1, ks = s & 1;
        if constexpr (T == 0) {
            ax = LB + slot_of(ch) + (ax0 ^ (ks * 64)); aw = LB + slot_of(ch) + aw0 + ks * 9216;
            asm volatile("" : "+v"(ax), "+v"(aw));
            wf[ks][0] = trfrag(aw, 2304); xf[0][s] = ld128(ax);
        } else if constexpr (T == 1) { xf[1][s] = ld128(ax + 2048); wf[ks][1] = trfrag(aw + 32, 2304); }
        else if constexpr (T == 2) wf[ks][2] = trfrag(aw + 144, 2304);
        else if constexpr (T == 3) wf[ks][3] = trfrag(aw + 176, 2304);
        else {
            const unsigned a4 = a4base + (isgate ? (unsigned)(s * 128) : (unsigned)(slot_of(ch) + ks * 9216));
            wf[ks][4] = trfrag(a4, 2304);
        }
    };
    sfor<0, 5>([&](auto T_) { reads(IC<0>{}, T_); });
    sfor<0, 3>([&](auto I_) { issue_piece(IC<2>{}, I_); });
    {
        auto waddr = [&](int i) {
            int hh = i / 324, rem = i - hh * 324, d = rem / 18, f = rem - d * 18;
            int e = hh & 1, dd = 18 * e + d, hf = f >= 9, f9 = f - 9 * hf, u = f9 >> 2, mm = 8 * e + 4 * hf + (f9 & 3);
            int fr = (hh >> 1) * 3 + u;
            return dd < 32 ? (unsigned)(LB + L_WT0 + (fr * 64 + ((dd & 15) >> 2) * 16 + mm) * 16 + 2 * ((dd & 3) + 4 * (dd >> 4)))
                           : (unsigned)(LB + L_WT1 + (fr * 64 + mm) * 8 + 2 * (dd - 32));
        };
        *(LAS unsigned short*)(uintptr_t)waddr(tid) = (unsigned short)wp0;
        *(LAS unsigned short*)(uintptr_t)waddr(tid + 512) = (unsigned short)wp1;
        if (tid < 272) *(LAS unsigned short*)(uintptr_t)waddr(tid + 1024) = (unsigned short)wp2;
        if (tid < 8) *(LAS unsigned short*)(uintptr_t)(LB + L_WT1 + (((tid >> 2) * 3 + 2) * 64 + 32 + 4 * (tid & 3) + 1) * 8) = (unsigned short)0x3F80;
    }
    sfor<0, 18>([&](auto S_) {
        constexpr int s = decltype(S_)::value, ks = s & 1;
        constexpr int cb = (s + 1) / 2;
        sfor<0, 5>([&](auto T_) {
            constexpr int T = decltype(T_)::value;
            SB();
            acc[T][0] = mfma(wf[ks][T], xf[0][s], acc[T][0]); acc[T][1] = mfma(wf[ks][T], xf[1][s], acc[T][1]);
            SB();
            if constexpr (T == 0 && s + 1 < 18 && (s + 1) % 2 == 0) {
                waitv<(cb <= 7 ? 6 : 4)>();
                BAR();
                if constexpr (cb == 4 || cb == 8) STAMP(cb == 4 ? 10 : 11);
            }
            if constexpr (s + 1 < 18) reads(IC<s + 1>{}, T_);
            if constexpr (T >= 1 && T < 4) {
                constexpr int i = T - 1, cn = cb + 2, half = ks ? 0 : 1;
                if constexpr (cn < 9) issue_piece(IC<cn>{}, IC<3 * half + i>{});
                else if constexpr (i < 2) issue_wq(IC<2 * (s - 13) + i>{});
            }
        });
    });
    SB();
    STAMP(2);
    BAR();

    auto feature_map = [&](const f4* t0, const f4* t1, const f4* t2, f4 (*P)[3]) {
        bf8 A0[3]; bf4 A1[3];
#pragma unroll
        for (int u = 0; u < 3; ++u) { A0[u] = ld128(LB + L_WT0 + ((hp * 3 + u) * 64 + lane) * 16); A1[u] = ld64(LB + L_WT1 + ((hp * 3 + u) * 64 + lane) * 8); }
#pragma unroll
        for (int tt = 0; tt < 2; ++tt) {
            const bf8 B0 = pack8(t0[tt], t1[tt]); const bf4 B1 = pack4(t2[tt]);
#pragma unroll
            for (int u = 0; u < 3; ++u) P[tt][u] = mfma16(A1[u], B1, mfma(A0[u], B0, (f4){0, 0, 0, 0}));
        }
    };
    float glogit[2];
    {
        f4 P[2][3];
        feature_map(acc[0], acc[1], acc[4], P);
        issue_wq(IC<10>{}); issue_wq(IC<11>{});
        const unsigned av = LB + L_V + (m * 32 + c) * V_P + (9 * hp + g) * 8;
        const unsigned av4 = LB + L_V + (m * 32 + c) * V_P + (g == 1 ? (9 * hp + 8) * 8 : 144);
#pragma unroll
        for (int tt = 0; tt < 2; ++tt) {
            *(LAS bf4*)(uintptr_t)(av + tt * 16 * V_P) = pack4(acc[2][tt]);
            *(LAS bf4*)(uintptr_t)(av + tt * 16 * V_P + 32) = pack4(acc[3][tt]);
            u2 val = __builtin_bit_cast(u2, pack4(acc[4][tt]));
            if (g != 1) val = (u2){0x3F80u, 0u};
            *(LAS u2*)(uintptr_t)(av4 + tt * 16 * V_P) = val;
        }
        issue_wq(IC<12>{}); issue_wq(IC<13>{});
        const unsigned ar = LB + L_PHI + (m * 32 + c) * PHI_P + 144 * hp;
        const unsigned am = ar + (g >> 1) * 80 + (g & 1) * 32, at = ar + 64 + 4 * g;
        sfor<0, 2>([&](auto TT_) {
            constexpr int tt = decltype(TT_)::value;
            glogit[tt] = P[tt][2][1];
            unsigned ph[9];
            dual_softmax(P[tt][0], P[tt][1], P[tt][2][0], ph);
            *(LAS u4*)(uintptr_t)(am + tt * 16 * PHI_P) = (u4){ph[0], ph[1], ph[2], ph[3]};
            *(LAS u4*)(uintptr_t)(am + tt * 16 * PHI_P + 16) = (u4){ph[4], ph[5], ph[6], ph[7]};
            *(LAS unsigned*)(uintptr_t)(at + tt * 16 * PHI_P) = ph[8];
            issue_wq(IC<14 + 2 * tt>{}); issue_wq(IC<15 + 2 * tt>{});
        });
    }
    BAR();
    STAMP(3);
    {
        const int hh = w >> 1, fh = w & 1;
        const int rpos = 16 * hrow + 2 * jrow;
        const unsigned aF = LB + L_PHI + rpos * PHI_P + 144 * (hh >> 1) + ((hh & 1) ? 80 : 0) + 32 * fh + 8 * p;
        const unsigned aF2 = LB + L_PHI + rpos * PHI_P + 144 * (p >> 1) + 64 + 8 * (p & 1);
        unsigned aD = LB + L_V + rpos * V_P + (9 * (hh >> 1) + ((hh & 1) ? 5 : 0)) * 8 + 8 * p;
        unsigned aDc = LB + L_V + rpos * V_P + (p == 0 ? 32 : (p == 1 ? 104 : 128 + 8 * p));
        asm volatile("" : "+v"(aD), "+v"(aDc));
        f4 ca = {0, 0, 0, 0}, cb = {0, 0, 0, 0}, cc = {0, 0, 0, 0};
#pragma unroll
        for (int mm = 0; mm < 4; ++mm) {
            bf8 F = trfrag(aF + mm * 32 * PHI_P, PHI_P), F2 = trfrag(aF2 + mm * 32 * PHI_P, PHI_P);
            bf8 Dh = trfrag(aD + mm * 32 * V_P, V_P), Dc = trfrag(aDc + mm * 32 * V_P, V_P);
            ca = mfma(F, Dh, ca); cb = mfma(F, Dc, cb); cc = mfma(F2, fh ? Dc : Dh, cc);
        }
        waitv<0>();
        char* p1 = ws + WS_P1 + (size_t)(cg * 16 + rt) * P1_UNIT;
        auto put = [&](int Q, f4 v) {
            gst16_sc1(Q * 16, p1, (u4){__float_as_uint(v[0]), tag, __float_as_uint(v[1]), tag});
            gst16_sc1(Q * 16 + 684 * 16, p1, (u4){__float_as_uint(v[2]), tag, __float_as_uint(v[3]), tag});
        };
        put(w * 64 + lane, ca);
        const int c0 = 4 * (hh >> 1) + 2 * (hh & 1);
        const int ci = (c == 8) ? 2 : c - c0;
        const bool cval = (c == 8) || (c == c0) || (c == c0 + 1);
        if (cval) put(512 + w * 12 + g * 3 + ci, cb);
        if (g == hh && (fh == 0 || cval)) put(fh == 0 ? 608 + hh * 16 + c : 672 + hh * 3 + ci, cc);
    }
    BAR();
    STAMP(4);
    const int tau = xcd * 30 + bi;
    const bool has2 = bi < 30 && tau < 234;
    const int rg = has2 ? tau / 9 : 0, nt = has2 ? tau - rg * 9 : 0;
    const int rot2 = nt & 7;
    unsigned vm, vl;
    {
        const int j8 = lane >> 3, ch = lane & 7, kk = 16 * ((w >> 1) & 1) + 8 * (j8 >> 2) + 4 * (w & 1) + (j8 & 3);
        const int cb = nt * 128 + (((ch >> 1) ^ ((j8 >> 1) & 3)) * 32) + (ch & 1) * 16;
        vm = (unsigned)((18 * (kk >> 4) + (kk & 15)) * XROWB + cb);
        vl = (unsigned)((18 * (2 * (2 * (kk >> 3) + ((kk & 7) >> 2)) + ((kk >> 1) & 1)) + 16 + (kk & 1)) * XROWB + cb);
    }
    auto issue_wo = [&](auto U_) {
        constexpr int u = decltype(U_)::value, j = u >> 1, kp = u & 1;
        const char* wo = kp ? wob : woa;
        if constexpr (j < 8) dma16<wo_addr(u)>(vm, wo + (size_t)(8 * hp + ((j + rot2) & 7)) * 36 * XROWB, lw1024);
        else dma16<wo_addr(u)>(vl, wo + (size_t)hp * 288 * XROWB, lw1024);
    };
    f4 qa[3][2];
#pragma unroll
    for (int T = 0; T < 3; ++T) { qa[T][0] = (f4){0, 0, 0, 0}; qa[T][1] = (f4){0, 0, 0, 0}; }
    {
        unsigned aq = LB + L_WQ + 9 * 4608 + (hrow * 8 + jrow) * 288 + 72 * hp + 8 * p;
        unsigned aq8 = LB + L_WQ + 9 * 4608 + (hrow * 8 + jrow) * 288 + 72 * hp + 64;
        asm volatile("" : "+v"(aq), "+v"(aq8));
        const int nq = rt == 15 ? 39 : 43;
        const int Q = rt * 43 + (lane < nq ? lane : 0);
        const char* p1 = ws + WS_P1 + (size_t)(cg * 16 + 2 * w) * P1_UNIT;
        u4 a0, a1, b0, b1;
        auto hop1_load = [&]() {
            a0 = gld16_sc1(Q * 16, p1); a1 = gld16_sc1(Q * 16 + 684 * 16, p1);
            b0 = gld16_sc1(Q * 16 + P1_UNIT, p1); b1 = gld16_sc1(Q * 16 + 684 * 16 + P1_UNIT, p1);
        };
        bf8 wqf[2][3];
        auto qreads = [&](auto S_, auto T_) {
            constexpr int s = decltype(S_)::value, T = decltype(T_)::value;
            if constexpr (s == 9 && T == 0) { aq -= 9 * 4608; aq8 -= 9 * 4608; asm volatile("" : "+v"(aq), "+v"(aq8)); }
            constexpr int o = ((17 - s) % 9) * 4608;
            if constexpr (T < 2) wqf[s & 1][T] = trfrag(aq + o + T * 32, 144); else wqf[s & 1][2] = trfrag(aq8 + o, 144);
        };
        sfor<0, 3>([&](auto T_) { qreads(IC<0>{}, T_); });
        sfor<0, 18>([&](auto S_) {
            constexpr int s = decltype(S_)::value;
            if constexpr (s == HOP1_ISSUE) hop1_load();
            if constexpr (s == HOP1_CHECK) {
                STAMP(5);
                asm volatile("s_waitcnt vmcnt(0)" : "+v"(a0), "+v"(a1), "+v"(b0), "+v"(b1) :: "memory");
                for (unsigned spins = 0;; ++spins) {
                    bool ok = a0[1] == tag && a0[3] == tag && a1[1] == tag && a1[3] == tag && b0[1] == tag && b0[3] == tag && b1[1] == tag && b1[3] == tag;
                    if (__all(ok) || spins > SPIN_CAP) break;
                    __builtin_amdgcn_s_sleep(1);
                    hop1_load();
                    asm volatile("s_waitcnt vmcnt(0)" : "+v"(a0), "+v"(a1), "+v"(b0), "+v"(b1) :: "memory");
                }
                f4 sum = {__uint_as_float(a0[0]) + __uint_as_float(b0[0]), __uint_as_float(a0[2]) + __uint_as_float(b0[2]),
                          __uint_as_float(a1[0]) + __uint_as_float(b1[0]), __uint_as_float(a1[2]) + __uint_as_float(b1[2])};
                if (lane < 43) *(LAS f4*)(uintptr_t)(LB + L_RED + (w * 43 + lane) * 16) = sum;
                BAR();
                if (w == 0 && lane < nq) {
                    f4 t = *(LAS f4*)(uintptr_t)(LB + L_RED + lane * 16);
#pragma unroll
                    for (int i = 1; i < 8; ++i) t += *(LAS f4*)(uintptr_t)(LB + L_RED + (i * 43 + lane) * 16);
                    gst16_sc1(Q * 16, ws + WS_P2 + (size_t)cg * P2_UNIT, (u4){pack2(t[0], t[1]), tag, pack2(t[2], t[3]), tag});
                }
                STAMP(6);
            }
            sfor<0, 3>([&](auto T_) {
                constexpr int T = decltype(T_)::value;
                SB();
                qa[T][0] = mfma(wqf[s & 1][T], xf[0][s], qa[T][0]); qa[T][1] = mfma(wqf[s & 1][T], xf[1][s], qa[T][1]);
                SB();
                if constexpr (s + 1 < 18) qreads(IC<s + 1>{}, T_);
                if constexpr (s < 6 && T == 0) issue_wo(IC<s>{});
                else if constexpr (s >= HOP1_CHECK && T == 0) issue_wo(IC<6 + (s - HOP1_CHECK)>{});
            });
        });
    }
    constexpr int WB0 = 6 + 18 - HOP1_CHECK;
    static_assert(WB0 <= 12, "");
    SB();
    STAMP(7);
    BAR();
    unsigned phq[2][9];
    {
        f4 P[2][3];
        feature_map(qa[0], qa[1], qa[2], P);
        sfor<WB0, WB0 + 3>([&](auto G_) { issue_wo(G_); });
        dual_softmax(P[0][0], P[0][1], P[0][2][0], phq[0]);
        SB();
        sfor<WB0 + 3, 17>([&](auto G_) { issue_wo(G_); });
        dual_softmax(P[1][0], P[1][1], P[1][2][0], phq[1]);
        SB();
    }
    STAMP(8);
    {
        const char* p2 = ws + WS_P2 + (size_t)cg * P2_UNIT;
        const int Q0 = tid, Q1 = tid < 172 ? 512 + tid : tid;
        u4 a0, a1;
        for (unsigned spins = 0;; ++spins) {
            a0 = gld16_sc1(Q0 * 16, p2); a1 = gld16_sc1(Q1 * 16, p2);
            asm volatile("s_waitcnt vmcnt(0)" : "+v"(a0), "+v"(a1) :: "memory");
            bool ok = a0[1] == tag && a0[3] == tag && a1[1] == tag && a1[3] == tag;
            if (__all(ok) || spins > SPIN_CAP) break;
            __builtin_amdgcn_s_sleep(1);
        }
        auto putq = [&](int hh, int d, int fq, unsigned lo, unsigned hi) {
            const int r0 = d < 16 ? (d >> 3) * 12 + (d & 7) : d - 8, r1 = d < 16 ? r0 : d + 4;
            const unsigned base = LB + L_KVT + hh * 24 * 80 + fq * 8, step = fq == 8 ? 8 : 4;
            *(LAS unsigned*)(uintptr_t)(base + r0 * 80) = lo; *(LAS unsigned*)(uintptr_t)(base + r0 * 80 + step) = hi;
            *(LAS unsigned*)(uintptr_t)(base + r1 * 80) = lo; *(LAS unsigned*)(uintptr_t)(base + r1 * 80 + step) = hi;
        };
        putq(w >> 1, c + 2 * ((w >> 1) & 1), 4 * (w & 1) + g, a0[0], a0[2]);
        if (tid < 172) {
            int hh, d, fq;
            if (tid < 96) { int ww = tid / 12, r = tid % 12, rgi = r / 3, ci = r % 3; hh = ww >> 1; fq = 4 * (ww & 1) + rgi; d = ci == 2 ? 18 : ((hh & 1) ? ci : 16 + ci); }
            else if (tid < 160) { int r = tid - 96; hh = r >> 4; fq = 8; d = (r & 15) + 2 * (hh & 1); }
            else { int r = tid - 160; hh = r / 3; int ci = r % 3; fq = 8; d = ci == 2 ? 18 : ((hh & 1) ? ci : 16 + ci); }
            putq(hh, d, fq, a1[0], a1[2]);
        }
        BAR();
        issue_wo(IC<17>{});
    }
    STAMP(9);
    {
        const unsigned ak = LB + L_KVT + ((((c >> 3) == (g >> 1)) ? 2 * hp + (g >> 1) : 4) * 24 + ((c >> 2) & 1) * 12 + (c & 3)) * 80;
        bf8 A[3][2]; bf4 At[3];
#pragma unroll
        for (int u = 0; u < 3; ++u) {
            A[u][0] = ld128(ak + (g & 1) * 32 + u * 320); A[u][1] = ld128(ak + (g & 1) * 32 + u * 320 + 16);
            At[u] = ld64(ak + 64 + (g & 1) * 8 + u * 320);
        }
        const int P = br * 16 + hg * 2 + hp;
        char* ob = ws + WS_O + (size_t)(rt * 8 + 2 * m) * 32 * 1024;
        unsigned left[2];
#pragma unroll
        for (int tt = 0; tt < 2; ++tt) {
            const bf8 B0 = __builtin_bit_cast(bf8, (u4){phq[tt][0], phq[tt][1], phq[tt][2], phq[tt][3]});
            const bf8 B1 = __builtin_bit_cast(bf8, (u4){phq[tt][4], phq[tt][5], phq[tt][6], phq[tt][7]});
            const bf4 B2 = __builtin_bit_cast(bf4, (u2){phq[tt][8], 0u});
            f4 nu[3];
#pragma unroll
            for (int u = 0; u < 3; ++u) nu[u] = mfma16(At[u], B2, mfma(A[u][1], B1, mfma(A[u][0], B0, (f4){0, 0, 0, 0})));
            const float gt = __builtin_amdgcn_rcpf(1.f + __builtin_amdgcn_exp2f(-1.4426950408889634f * glogit[tt]));
            const float sc = (br ? 1.f - gt : gt) * __builtin_amdgcn_rcpf(nu[2][2] + 1e-6f);
            gst16_sc1(tt * 32 * 1024 + P * 1024 + lane * 16, ob, __builtin_bit_cast(u4, pack8(nu[0] * sc, nu[1] * sc)));
            left[tt] = pack2(nu[2][0] * sc, nu[2][1] * sc);
        }
        {
            auto r = __builtin_amdgcn_permlane32_swap(left[0], left[1], false, false);
            if (!(g & 1)) gst8_sc1((unsigned)((((rt * 8 + 2 * m + (g >> 1)) * 32 + P) * 16 + c) * 8), ws + WS_OL, (u2){r[0], r[1]});
        }
        waitv<0>();
        if (lane == 0) gst4_sc1(((rt * 4 + m) * 32 + P) * 4, ws + WS_FLAG, tag);
    }
    STAMP(12);
    BAR();
    if (has2) {
        const int kq2 = w & 3, mh = w >> 2;
        const int T0 = rg * 5 + 3 * mh;
        const int Tc0 = T0 < 127 ? T0 : 127, Tc1 = T0 + 1 < 127 ? T0 + 1 : 127, Tc2 = T0 + 2 < 127 ? T0 + 2 : 127;
        {
            const int Tl = mh ? Tc1 : Tc2;
            const unsigned fo = (unsigned)((((lane & 8) ? (Tl >> 1) : (Tc0 >> 1)) * 32 + 8 * kq2 + (lane & 7)) * 4);
            for (unsigned spins = 0;; ++spins) {
                unsigned f = gld4_sc1(fo, ws + WS_FLAG);
                asm volatile("s_waitcnt vmcnt(0)" : "+v"(f) :: "memory");
                if (__all(f == tag) || spins > SPIN_CAP) break;
                __builtin_amdgcn_s_sleep(1);
            }
        }
        STAMP(13);
        const char* ob = ws + WS_O + (size_t)kq2 * 8 * 1024;
        const char* ol = ws + WS_OL + (size_t)kq2 * 8 * 128;
        const unsigned vo[3] = {(unsigned)(Tc0 * 32768 + lane * 16), (unsigned)(Tc1 * 32768 + lane * 16), (unsigned)(Tc2 * 32768 + lane * 16)};
        const unsigned vol[3] = {(unsigned)(Tc0 * 4096 + g * 256 + c * 8), (unsigned)(Tc1 * 4096 + g * 256 + c * 8), (unsigned)(Tc2 * 4096 + g * 256 + c * 8)};
        unsigned au[4];
#pragma unroll
        for (int u = 0; u < 4; ++u) au[u] = LB + kq2 * 4096 + hrow * 2048 + jrow * 128 + ((((u + kq2) & 3) ^ (jrow >> 1)) * 32) + 8 * p;
        const int last_off = kq2 < 2 ? wo_addr(16) : wo_addr(17) - 8192;
        f4 oc[3][4];
        auto gemm2 = [&](auto NT_) {
            constexpr int NTL = decltype(NT_)::value;
            u4 of[8][NTL]; u2 ofl[NTL][2];
            sfor<0, 8>([&](auto J_) {
                constexpr int j = decltype(J_)::value;
                const char* src = ob + ((j + rot2) & 7) * 1024;
#pragma unroll
                for (int t = 0; t < NTL; ++t) of[j][t] = gld16_sc1n(vo[t], src);
            });
#pragma unroll
            for (int t = 0; t < NTL; ++t) { ofl[t][0] = gld8_sc1o<0>(vol[t], ol); ofl[t][1] = gld8_sc1o<128>(vol[t], ol); }
#pragma unroll
            for (int t = 0; t < NTL; ++t)
#pragma unroll
                for (int u = 0; u < 4; ++u) oc[t][u] = (f4){0, 0, 0, 0};
            bf8 wn[2][4];
            auto oreads = [&](auto J_) {
                constexpr int j = decltype(J_)::value;
#pragma unroll
                for (int u = 0; u < 4; ++u) { unsigned a = au[u] + (j < 8 ? wo_addr(2 * j) : last_off); asm volatile("" : "+v"(a)); wn[j & 1][u] = trfrag(a, 1024); }
            };
            oreads(IC<0>{});
            sfor<0, 9>([&](auto J_) {
                constexpr int j = decltype(J_)::value;
                SB();
                if constexpr (j + 1 < 9) oreads(IC<j + 1>{});
                sfor<0, NTL>([&](auto T_) {
                    constexpr int t = decltype(T_)::value;
                    bf8 B;
                    if constexpr (j < 8) {
                        asm volatile("s_waitcnt vmcnt(%1)" : "+v"(of[j][t]) : "n"((7 - j) * NTL + (NTL - 1 - t) + 2 * NTL) : "memory");
                        B = __builtin_bit_cast(bf8, of[j][t]);
                    } else {
                        asm volatile("s_waitcnt vmcnt(%2)" : "+v"(ofl[t][0]), "+v"(ofl[t][1]) : "n"(2 * (NTL - 1 - t)) : "memory");
                        B = __builtin_bit_cast(bf8, (u4){ofl[t][0][0], ofl[t][0][1], ofl[t][1][0], ofl[t][1][1]});
                    }
#pragma unroll
                    for (int u = 0; u < 4; ++u) oc[t][u] = mfma(wn[j & 1][u], B, oc[t][u]);
                });
            });
            SB();
        };
        if (mh == 0) gemm2(IC<3>{});
        else {
            gemm2(IC<2>{});
#pragma unroll
            for (int u = 0; u < 4; ++u) oc[2][u] = (f4){0, 0, 0, 0};
        }
        STAMP(14);
        BAR();
        const int ntl = 3 - mh;
#pragma unroll
        for (int t = 0; t < 3; ++t)
#pragma unroll
            for (int u = 1; u < 4; ++u)
                if (t < ntl) *(LAS f4*)(uintptr_t)(LB + (((kq2 * 5 + 3 * mh + t) * 4 + ((u + kq2) & 3)) * 64 + lane) * 16) = oc[t][u];
        BAR();
#pragma unroll
        for (int t = 0; t < 3; ++t) {
            if (t < ntl && T0 + t < 128) {
                f4 v = oc[t][0];
#pragma unroll
                for (int sq = 1; sq < 4; ++sq) v += *(LAS f4*)(uintptr_t)(LB + (((((sq + kq2) & 3) * 5 + 3 * mh + t) * 4 + kq2) * 64 + lane) * 16);
                *(bf4*)(out + (size_t)((T0 + t) * 16 + c) * XROWB + nt * 128 + kq2 * 32 + g * 8) = pack4(v);
            }
        }
        if (b == 0 && tid == 0) gst4_sc1(0, ws + WS_EPOCH, tag);
    }
    STAMP(15);
    STAMP_OUT(NSTAMPS);
    asm volatile("Lkf_end:");
}

extern "C" void kernel_launch(void* const* d_in, const int* in_sizes, int n_in,
                              void* d_out, int out_size, void* d_ws, size_t ws_size,
                              hipStream_t stream) {
    kf<<<256, 512, 0, stream>>>((const char*)d_in[0], (const char*)d_in[1], (const char*)d_in[5], (const char*)d_in[2], (const char*)d_in[6],
                                (const char*)d_in[8], (const char*)d_in[3], (const char*)d_in[7], (char*)d_ws, (char*)d_out);
}
```

```cpp
#include <hip/hip_runtime.h>
#include <hip/hip_bf16.h>
#include <stdint.h>

typedef __bf16 bf8 __attribute__((ext_vector_type(8)));
typedef __bf16 bf4 __attribute__((ext_vector_type(4)));
typedef __bf16 bf2 __attribute__((ext_vector_type(2)));
typedef float f4 __attribute__((ext_vector_type(4)));
typedef float f2 __attribute__((ext_vector_type(2)));
typedef unsigned u4 __attribute__((ext_vector_type(4)));
typedef unsigned u2 __attribute__((ext_vector_type(2)));
#define LAS __attribute__((address_space(3)))
#define DI __device__ __forceinline__

constexpr int XROWB = 1152, WROWB = 3456;
constexpr int L_WT0 = 0;
constexpr int L_WT1 = 6144;
constexpr int L_KVT = 9216;
constexpr int L_GATE = 18816;
constexpr int L_RING = 23424;
constexpr int SLOT = 34816;
constexpr int L_TOTAL = 163840;
constexpr int PHI_P = 304, V_P = 176;
constexpr int L_PHI = L_GATE;
constexpr int L_V = L_RING + SLOT;
constexpr int L_WQ = L_TOTAL - 18 * 4608;
constexpr int L_WO = L_GATE;
constexpr int L_XQ = L_WO + 17 * 8192;
constexpr int L_RED = L_WO + 6 * 8192;
static_assert(L_PHI + 128 * PHI_P <= L_V && L_V + 128 * V_P <= L_WQ && L_RED + 8 * 64 * 16 <= L_WQ && L_WO + 17 * 8192 <= L_TOTAL, "lds");
static_assert(L_WQ + 11 * 4608 >= L_RING + 3 * SLOT && L_WQ + 3 * 4608 >= L_RING + 2 * SLOT, "wq slabs");
constexpr int slot_of(int c) { return L_RING + ((8 - c) % 3) * SLOT; }
constexpr int wo_addr(int u) { return u < 6 ? L_WO + u * 8192 : (u < 12 ? L_WO + (u + 5) * 8192 : (u < 17 ? L_WO + (u - 6) * 8192 : 0)); }
constexpr size_t WS_EPOCH = 0;
constexpr size_t WS_FLAG = 4096;
constexpr size_t WS_P1 = 16384;
constexpr int PLANE = 768 * 16;
constexpr int P1_UNIT = PLANE;
constexpr size_t WS_P2 = WS_P1 + (size_t)256 * P1_UNIT;
constexpr int P2_UNIT = PLANE;
constexpr size_t WS_O = (WS_P2 + (size_t)16 * P2_UNIT + 4095) / 4096 * 4096;
constexpr size_t WS_OL = WS_O + (size_t)128 * 32 * 1024;
constexpr unsigned SPIN_CAP = 1u << 16;
constexpr int HOP1_ISSUE = 10, HOP1_CHECK = 16;
constexpr int ARM0 = 15;
constexpr int NEARLY = 5, ES = 4;
constexpr int OBUF = 2;
static_assert(L_WO + 11 * 8192 >= L_WQ + (18 - HOP1_CHECK) * 4608, "wo wave B needs these Wq slabs consumed");

DI const void* uni(const void* p) {
    uint64_t v = (uint64_t)p;
    unsigned lo = __builtin_amdgcn_readfirstlane((unsigned)v), hi = __builtin_amdgcn_readfirstlane((unsigned)(v >> 32));
    return (const void*)(((uint64_t)hi << 32) | lo);
}
template <int IMM> DI void dma16(unsigned voff, const void* sbase, unsigned ldsbase) {
    asm volatile("s_add_u32 m0, %2, %3\n\ts_nop 0\n\tglobal_load_lds_dwordx4 %0, %1"
                 :: "v"(voff), "s"(sbase), "s"(ldsbase), "n"(IMM) : "memory", "scc");
}
template <int IMM> DI void dma16m(unsigned voff, const void* sbase, unsigned ldsbase, uint64_t mask) {
    asm volatile("s_add_u32 m0, %2, %3\n\ts_mov_b64 exec, %4\n\tglobal_load_lds_dwordx4 %0, %1\n\ts_mov_b64 exec, -1"
                 :: "v"(voff), "s"(sbase), "s"(ldsbase), "n"(IMM), "s"(mask) : "memory", "scc");
}
DI void gst16_sc1n(unsigned voff, void* sbase, u4 v) { asm volatile("global_store_dwordx4 %0, %1, %2 sc1\n\ts_nop 1" :: "v"(voff), "v"(v), "s"(sbase) : "memory"); }
DI void gst8_sc1n(unsigned voff, void* sbase, u2 v) { asm volatile("global_store_dwordx2 %0, %1, %2 sc1" :: "v"(voff), "v"(v), "s"(sbase) : "memory"); }
DI void gst4_sc1n(unsigned voff, void* sbase, unsigned v) { asm volatile("global_store_dword %0, %1, %2 sc1" :: "v"(voff), "v"(v), "s"(sbase) : "memory"); }
DI unsigned gld4_sc1n(unsigned voff, const void* sbase) { unsigned r; asm volatile("global_load_dword %0, %1, %2 sc1" : "=v"(r) : "v"(voff), "s"(sbase) : "memory"); return r; }
DI unsigned tagdiff(unsigned a, unsigned tag, unsigned acc) { return __builtin_amdgcn_bitop3_b32(a, tag, acc, 0xBE); }
DI u4 gld16_sc1n(unsigned voff, const void* sbase) { u4 r; asm volatile("global_load_dwordx4 %0, %1, %2 sc1" : "=v"(r) : "v"(voff), "s"(sbase) : "memory"); return r; }
template <int OFF> DI u2 gld8_sc1o(unsigned voff, const void* sbase) { u2 r; asm volatile("global_load_dwordx2 %0, %1, %2 offset:%3 sc1" : "=v"(r) : "v"(voff), "s"(sbase), "n"(OFF) : "memory"); return r; }
DI unsigned gld4(unsigned voff, const void* sbase) { unsigned r; asm volatile("global_load_dword %0, %1, %2" : "=v"(r) : "v"(voff), "s"(sbase) : "memory"); return r; }
template <int OFF> DI unsigned gldu16(unsigned voff, const void* sbase) { unsigned r; asm volatile("global_load_ushort %0, %1, %2 offset:%3" : "=v"(r) : "v"(voff), "s"(sbase), "n"(OFF) : "memory"); return r; }
DI void gst4_sc1(unsigned voff, void* sbase, unsigned v) { asm volatile("s_nop 4\n\tglobal_store_dword %0, %1, %2 sc1" :: "v"(voff), "v"(v), "s"(uni(sbase)) : "memory"); }
#define BAR() do { asm volatile("s_waitcnt lgkmcnt(0)" ::: "memory"); __builtin_amdgcn_s_barrier(); asm volatile("" ::: "memory"); } while (0)
template <int N> DI void waitv() { asm volatile("s_waitcnt vmcnt(%0)" :: "n"(N) : "memory"); }
template <int N, class A> DI void waitr(A& a, A& b) { asm volatile("s_waitcnt vmcnt(%2)" : "+v"(a), "+v"(b) : "n"(N) : "memory"); }
template <int N, class A> DI void waitr(A& a, A& b, A& c) { asm volatile("s_waitcnt vmcnt(%3)" : "+v"(a), "+v"(b), "+v"(c) : "n"(N) : "memory"); }
#define SB() __builtin_amdgcn_sched_barrier(0)
template <int V> struct IC { static constexpr int value = V; };
template <int I, int N, class F> DI void sfor(F&& f) { if constexpr (I < N) { f(IC<I>{}); sfor<I + 1, N>(f); } }

DI bf4 trd(unsigned a) { return __builtin_amdgcn_ds_read_tr16_b64_v4bf16((LAS bf4*)(uintptr_t)a); }
DI bf8 trfrag(unsigned a, int toff) { bf4 lo = trd(a), hi = trd(a + toff); return __builtin_shufflevector(lo, hi, 0, 1, 2, 3, 4, 5, 6, 7); }
DI bf8 ld128(unsigned a) { return *(LAS bf8*)(uintptr_t)a; }
DI bf4 ld64(unsigned a) { return *(LAS bf4*)(uintptr_t)a; }
DI f4 mfma(bf8 a, bf8 b, f4 c) { return __builtin_amdgcn_mfma_f32_16x16x32_bf16(a, b, c, 0, 0, 0); }
DI f4 mfma16(bf4 a, bf4 b, f4 c) { const bf4 z = __builtin_bit_cast(bf4, (u2){0u, 0u}); return mfma(__builtin_shufflevector(a, z, 0, 1, 2, 3, 4, 5, 6, 7), __builtin_shufflevector(b, z, 0, 1, 2, 3, 4, 5, 6, 7), c); }
DI bf4 pack4(f4 v) { return __builtin_convertvector(v, bf4); }
DI bf8 pack8(f4 a, f4 b) { return __builtin_shufflevector(pack4(a), pack4(b), 0, 1, 2, 3, 4, 5, 6, 7); }
DI unsigned pack2(float a, float b) { f2 v = {a, b}; return __builtin_bit_cast(unsigned, __builtin_convertvector(v, bf2)); }
DI unsigned pack2v(f2 v) { return __builtin_bit_cast(unsigned, __builtin_convertvector(v, bf2)); }
DI f2 swap16(float x) { auto r = __builtin_amdgcn_permlane16_swap(__float_as_uint(x), __float_as_uint(x), false, false); return (f2){__uint_as_float(r[0]), __uint_as_float(r[1])}; }


DI float vmax(float a, float b) { float r; asm("v_max_f32 %0, %1, %2" : "=v"(r) : "v"(a), "v"(b)); return r; }
template <bool NORM, class H1, class H2> DI void dual_softmax(f4 p0, f4 p1, float p2, unsigned* ph, float* s1out, H1&& hook1, H2&& hook2) {
    float mx = fmaxf(fmaxf(p0[0], p0[1]), p2), nm = fmaxf(fmaxf(-p0[0], -p0[1]), -p2);
    mx = fmaxf(fmaxf(mx, p0[2]), p0[3]); nm = fmaxf(fmaxf(nm, -p0[2]), -p0[3]);
    mx = fmaxf(fmaxf(mx, p1[0]), p1[1]); nm = fmaxf(fmaxf(nm, -p1[0]), -p1[1]);
    mx = fmaxf(fmaxf(mx, p1[2]), p1[3]); nm = fmaxf(fmaxf(nm, -p1[2]), -p1[3]);
    const float L2E = 1.4426950408889634f;
    float a, b;
    {
        auto r = __builtin_amdgcn_permlane16_swap(__float_as_uint(mx), __float_as_uint(nm), false, false);
        const f2 t = swap16(-L2E * vmax(__uint_as_float(r[0]), __uint_as_float(r[1])));
        a = t[0]; b = t[1];
    }
    f2 x[4] = {{p0[0], p0[1]}, {p0[2], p0[3]}, {p1[0], p1[1]}, {p1[2], p1[3]}};
    f2 ep[4], en[4];
#pragma unroll
    for (int i = 0; i < 4; ++i) {
        if (i == 1) hook1();
        f2 tp = x[i] * (f2){L2E, L2E} + (f2){a, a};
        f2 tn = x[i] * (f2){-L2E, -L2E} + (f2){b, b};
        ep[i] = (f2){__builtin_amdgcn_exp2f(tp[0]), __builtin_amdgcn_exp2f(tp[1])};
        en[i] = (f2){__builtin_amdgcn_exp2f(tn[0]), __builtin_amdgcn_exp2f(tn[1])};
    }
    f2 t8 = (f2){p2, p2} * (f2){L2E, -L2E} + (f2){a, b};
    f2 e8 = {__builtin_amdgcn_exp2f(t8[0]), __builtin_amdgcn_exp2f(t8[1])};
    hook2();
    f2 sp = (ep[0] + ep[1]) + (ep[2] + ep[3]), sn = (en[0] + en[1]) + (en[2] + en[3]);
    const float s1 = (sp[0] + sp[1]) + e8[0], s2 = (sn[0] + sn[1]) + e8[1];
    auto r = __builtin_amdgcn_permlane16_swap(__float_as_uint(s1), __float_as_uint(s2), false, false);
    const float sg = __uint_as_float(r[0]) + __uint_as_float(r[1]);
    if constexpr (NORM) {
        const f2 t = swap16(__builtin_amdgcn_rcpf(sg));
        const float rp = t[0], rn = t[1];
#pragma unroll
        for (int i = 0; i < 4; ++i) { ph[i] = pack2v(ep[i] * (f2){rp, rp}); ph[4 + i] = pack2v(en[i] * (f2){rn, rn}); }
        ph[8] = pack2v(e8 * t);
    } else {
        const f2 t = swap16(sg);
        const float rho = t[0] * __builtin_amdgcn_rcpf(t[1]);
        *s1out = t[0];
#pragma unroll
        for (int i = 0; i < 4; ++i) { ph[i] = pack2v(ep[i]); ph[4 + i] = pack2v(en[i] * (f2){rho, rho}); }
        ph[8] = pack2(e8[0], e8[1] * rho);
    }
}

__global__ __launch_bounds__(512, 2) void kf(const char* __restrict__ x, const char* __restrict__ wqa, const char* __restrict__ wqb,
                                             const char* __restrict__ wpa, const char* __restrict__ wpb, const char* __restrict__ gw,
                                             const char* __restrict__ woa, const char* __restrict__ wob, char* ws, char* out) {
    __shared__ __attribute__((aligned(16))) char lds[L_TOTAL];
    const int tid = threadIdx.x, lane = tid & 63, w = __builtin_amdgcn_readfirstlane(tid >> 6);
    const int b = blockIdx.x, xcd = b & 7, bi = b >> 3;
    unsigned cpf0;
    {
        uint64_t pc; unsigned len;
        asm volatile("s_getpc_b64 %0\n\tLkf_here%=:\n\ts_mov_b32 %1, Lkf_end - Lkf_here%=" : "=s"(pc), "=s"(len));
        unsigned off = (unsigned)((bi * 8 + w) * 128);
        off = off + 4 <= len ? off : 0;
        asm volatile("global_load_dword %0, %1, %2" : "=v"(cpf0) : "v"(off), "s"(pc) : "memory");
        SB();
    }
    const int rt = (xcd & 1) * 8 + (bi & 7), cg = (xcd >> 1) * 4 + (bi >> 3);
    const int br = cg >> 3, hg = cg & 7;
    const int m = w & 3, hp = w >> 2;
    const int c = lane & 15, g = lane >> 4, q = c >> 2, p = lane & 3;
    const unsigned LB = (unsigned)(uintptr_t)(LAS char*)lds;
    const uint64_t M36 = (1ull << 36) - 1;
    const int rot = ((bi & 7) + 2 * (bi >> 3)) % 9;

    unsigned vx = (unsigned)((w * 8 + (lane >> 3)) * XROWB + (((lane & 7) ^ ((4 * (w & 1) + (lane >> 4)) & 7)) * 16));
    unsigned vw, vq;
    {
        const int l36 = lane < 36 ? lane : 0;
        const int lr = l36 >= 18, lci = l36 - 18 * lr, lsec = lci >= 9, lcc = lci - 9 * lsec;
        const int pq = 4 * w + 2 * lr + lsec;
        const int kq = 16 * (pq >> 4) + 8 * ((pq >> 3) & 1) + 4 * (pq & 1) + ((pq >> 1) & 3);
        const int pos = 2 * w + lr, t = pos >> 3, j = pos & 7;
        vw = (unsigned)((8 * (j >> 2) + 4 * t + (j & 3)) * WROWB + (576 + 576 * lsec) * 2 + hg * 144 + lcc * 16);
        vq = (unsigned)(kq * WROWB + hg * 144 + lcc * 16);
    }
    unsigned lw1024 = LB + w * 1024, lw576 = LB + w * 576, xo0 = rt * 128 * XROWB, rot_ = rot;
    {
        const u4 z = {0, 0, 0, 0};
        *(LAS u4*)(uintptr_t)(LB + tid * 16) = z;
        *(LAS u4*)(uintptr_t)(LB + (tid + 512) * 16) = z;
        *(LAS u4*)(uintptr_t)(LB + (tid < 152 ? tid + 1024 : tid) * 16) = z;
    }
    const int jrow = 4 * (g & 1) + q, hrow = g >> 1;
    const unsigned ax0 = (unsigned)((32 * m + 16 * hp + c) * 128 + ((g ^ (c >> 1)) * 16));
    const unsigned aw0 = (unsigned)(16384 + (16 * hrow + jrow) * 288 + 72 * hp + 8 * p);
    const unsigned aw4 = (unsigned)(16384 + (16 * hrow + jrow) * 288 + 64 + 144 * (p & 1) + 72 * (p >> 1));
    const unsigned ag = LB + L_GATE + g * 16;
    asm volatile("" : "+v"(vx), "+v"(vw), "+s"(lw1024), "+s"(lw576), "+s"(xo0), "+s"(rot_));
    SB();
    const char* wq = br ? wqb : wqa;
    const char* wp = (br ? wpb : wpa) + hg * 4 * 324 * 2;
    const char* xb = x + xo0;
    const char* xb1 = xb + 64 * XROWB;
    const char* wq1 = wq + 16 * WROWB; const char* wq2 = wq + 32 * WROWB; const char* wq3 = wq + 48 * WROWB;
    unsigned sx = rot * 128, vxc, vwc;
    auto issue_piece = [&](auto C_, auto I_) {
        constexpr int ch = decltype(C_)::value, i = decltype(I_)::value, slot = slot_of(ch);
        if constexpr (i == 0) { vxc = vx + sx; vwc = vw + sx * (64 * WROWB / 128); sx = sx == 8 * 128 ? 0 : sx + 128; }
        if constexpr (i < 2) dma16<slot + 8 * i * 1024>(vxc, i ? xb1 : xb, lw1024);
        else dma16m<slot + 16384 + (i - 2) * 4608>(vwc, i == 2 ? wq : (i == 3 ? wq1 : (i == 4 ? wq2 : wq3)), lw576, M36);
    };
    auto issue_wq = [&](auto S_) {
        constexpr int s = decltype(S_)::value;
        if constexpr ((s & 1) == 0) { vwc = vq + sx * (64 * WROWB / 128); sx = sx == 8 * 128 ? 0 : sx + 128; }
        dma16m<L_WQ + (17 - s) * 4608>(vwc, (s & 1) ? wq2 : wq, lw576, M36);
    };
    sfor<0, 6>([&](auto I_) { issue_piece(IC<0>{}, I_); });
    SB();
    const int we = (w >> 1) & 1, wf_ = lane & 31, wd0 = 2 * (w & 1) + (lane >> 5) - 2 * we;
    const int tg = tid < 288 ? tid : 0;
    unsigned gw0 = gld4(tg * 4, gw);
    unsigned agw;
    { int o = tg * 4 - rot * 128; o = o < 0 ? o + 1152 : o; agw = LB + L_GATE + o; asm volatile("" : "+v"(agw)); }
    unsigned wpv[5];
    {
        const unsigned o = (unsigned)(((w >> 1) * 324 + wd0 * 18 + (wf_ < 18 ? wf_ : 0)) * 2);
        sfor<0, 5>([&](auto K_) { wpv[decltype(K_)::value] = gldu16<decltype(K_)::value * 144>(o, wp); });
    }
    unsigned ep0 = gld4_sc1n(0, ws);
    unsigned cpf;
    {
        const char* a = woa;
        const int id = b * 8 + w;
        if (lane >= 16 && lane < 25 && id < 1152) a = (id >= 576 ? wqb + (id - 576) * WROWB : wqa + id * WROWB) + (lane - 16) * 128;
        if (lane >= 32 && lane < 41 && id >= 896) a = (id >= 1472 ? wob + (id - 1472) * XROWB : woa + (id - 896) * XROWB) + (lane - 32) * 128;
        asm volatile("global_load_dword %0, %1, off" : "=v"(cpf) : "v"(a) : "memory");
    }
    const int P = br * 16 + hg * 2 + hp;
    const unsigned vol_st = (unsigned)(WS_OL + (((rt * 8 + 2 * m + ((g >> 1) ^ hp)) * 32 + P) * 16 + c) * 8);
    const int tau = xcd * 30 + bi;
    const bool has2 = bi < 30 && tau < 234;
    const int rg = has2 ? tau / 9 : 0, nt = has2 ? tau - rg * 9 : 0;
    const int rot2 = (nt + 2 * rg) & 7;
    const int kq2 = w & 3, mh = w >> 2;
    const int T0 = rg * 5 + 3 * mh;
    const int Tc0 = T0 < 127 ? T0 : 127, Tc1 = T0 + 1 < 127 ? T0 + 1 : 127, Tc2 = T0 + 2 < 127 ? T0 + 2 : 127;
    const int hh = w >> 1, fh = w & 1, Tl = mh ? Tc1 : Tc2;
    unsigned pk_F, pk_D, pk_st, pk_x, pk_k01, pk_kh, pk_ph, pk_v, pk_s, pk_q, pk_wo, pk_au;
    unsigned wa0, wa1, wa2, wstep;
    unsigned so_p1w, so_p1r, so_p2w, so_p2r, so_fl, so_o, so_wo, so_wend;
    auto setup = [&](auto K_) __attribute__((always_inline)) {
        constexpr int k = decltype(K_)::value;
        auto pk = [](unsigned lo, unsigned hi) { return lo | (hi << 16); };
        const int rpos = 16 * hrow + 2 * jrow, odd = hh & 1;
        const unsigned stg = LB + L_KVT + (hh * 20 + fh * 8) * 80, spare = LB + L_KVT + 100 * 80 + w * 16;
        if constexpr (k < 3) {
            const int hf = wf_ >= 9, f9 = wf_ - 9 * hf, mm = 8 * we + 4 * hf + (f9 & 3), fr = (w >> 2) * 3 + (f9 >> 2), dd4 = wd0 + 2 * we;
            const bool ok = wf_ < 18;
            wa0 = ok ? (unsigned)(LB + L_WT0 + (fr * 64 + mm) * 16 + 2 * dd4 + 8 * we) : spare;
            const unsigned a4 = we ? (unsigned)(LB + L_WT1 + (fr * 64 + (w >> 2) * 32 + mm) * 8 + 2 * dd4) : wa0 + 8;
            wa1 = (ok & (we != 0 || dd4 < 2)) ? a4 : spare;
            wa2 = (we != 0 && dd4 < 2) ? spare : wa0;
            wstep = ok ? 256 : 0;
            asm volatile("" : "+v"(wa0), "+v"(wa1), "+v"(wa2), "+v"(wstep));
        } else if constexpr (k == 3) {
            pk_F = pk(LB + L_PHI + rpos * PHI_P + 144 * (hh >> 1) + (odd ? 80 : 0) + 32 * fh + 8 * p, LB + L_PHI + rpos * PHI_P + 144 * (p >> 1) + 64 + 8 * (p & 1));
            const unsigned aD = LB + L_V + rpos * V_P + (9 * (hh >> 1) + (odd ? 5 : 0)) * 8 + 8 * p;
            const unsigned aDc = LB + L_V + rpos * V_P + ((0x98906820u >> (8 * p)) & 0xff);
            pk_D = fh ? pk(aDc, aD) : pk(aD, aDc);
            asm volatile("" : "+v"(pk_F), "+v"(pk_D));
        } else if constexpr (k == 4) {
            const int ci = c >= 8 ? c - 6 : c - 2 * hh;
            const bool cval = ((c >> 1) == 4) | ((c >> 1) == hh);
            const unsigned sb_ = stg + g * 80 + ci * 16, sc_ = stg + (4 + q) * 80 + p * 16;
            pk_st = pk(cval ? sb_ : spare, g == hh ? sc_ : spare);
            pk_x = pk(stg + ((lane & 31) >> 2) * 80 + p * 16, stg + (lane >> 3) * 80 + (lane & 7) * 8);
            asm volatile("" : "+v"(pk_st), "+v"(pk_x));
        } else if constexpr (k == 5) {
            const int ci = g == 0 ? (c & 3) : (c == 8 ? 2 : c - 2 * hh);
            const int dsm = ci == 2 ? 18 : (odd ? ci : 16 + ci);
            const bool full = (g != 0) & (fh == 0);
            const int d = full ? c + 2 * odd : dsm, fq = g == 0 ? 4 * fh + (c >> 2) : 8;
            const bool val = (g < 2) & (full | (g == 0 ? ci < 3 : ((c == 8) | ((c >> 1) == hh))));
            pk_k01 = pk(LB + L_KVT + (hh * 20 + c + 2 * odd) * 80 + (4 * fh + g) * 8, val ? LB + L_KVT + (hh * 20 + d) * 80 + fq * 8 : spare);
            asm volatile("" : "+v"(pk_k01));
        } else if constexpr (k == 6) {
            const unsigned akh = LB + L_KVT + (((c >> 3) == (g >> 1)) ? 2 * hp + (g >> 1) : 4) * 1600 + (c & 3) * 80;
            pk_kh = pk(akh, akh + ((c >> 2) & 1) * 640);
            const unsigned ar = LB + L_PHI + (m * 32 + 16 * hp + c) * PHI_P + 144 * hp;
            pk_ph = pk(ar + (g >> 1) * 80 + (g & 1) * 32, ar + 64 + 4 * g);
            asm volatile("" : "+v"(pk_kh), "+v"(pk_ph));
        } else if constexpr (k == 7) {
            const unsigned av = (m * 32 + 16 * hp + c) * V_P;
            pk_v = pk(av + (9 * hp + g) * 8, av + ((g & 1) ? 64 + 72 * (g >> 1) : 144));
            pk_s = pk(1024 + (lane & 31) * 16, (((lane & 8) ? (Tl >> 1) : (Tc0 >> 1)) * 32 + 8 * kq2 + (lane & 7)) * 4);
            pk_q = pk((hrow * 8 + jrow) * 288 + 72 * hp + 8 * p, (hrow * 8 + jrow) * 288 + 72 * (p >> 1) + 64);
            asm volatile("" : "+v"(pk_v), "+v"(pk_s), "+v"(pk_q));
        } else if constexpr (k == 8) {
            const int j8 = lane >> 3, ch = lane & 7, kk = 16 * ((w >> 1) & 1) + 8 * (j8 >> 2) + 4 * (w & 1) + (j8 & 3);
            const int cb = nt * 128 + (((ch >> 1) ^ ((j8 >> 1) & 3)) * 32) + (ch & 1) * 16;
            pk_wo = pk((18 * (kk >> 4) + (kk & 15)) * XROWB + cb, ((18 * (2 * (2 * (kk >> 3) + ((kk & 7) >> 2)) + ((kk >> 1) & 1)) + 16 + (kk & 1)) * XROWB + cb) >> 4);
            pk_au = pk(LB + kq2 * 4096 + hrow * 2048 + jrow * 128 + ((jrow >> 1) * 32) + 8 * p, g * 256 + c * 8);
            asm volatile("" : "+v"(pk_wo), "+v"(pk_au));
        } else if constexpr (k == 9) {
            so_p1w = (unsigned)(WS_P1 + (size_t)(cg * 16 + rt) * P1_UNIT + w * 1536); so_p1r = (unsigned)(WS_P1 + (size_t)(cg * 16 + 2 * w) * P1_UNIT + rt * 768);
            so_p2w = (unsigned)(WS_P2 + (size_t)cg * P2_UNIT + rt * 768); so_p2r = (unsigned)(WS_P2 + (size_t)cg * P2_UNIT + w * 1536);
            so_fl = (unsigned)(WS_FLAG + ((rt * 4 + m) * 32 + P) * 4); so_o = (unsigned)(WS_O + (size_t)(rt * 8 + 2 * m) * 32 * 1024 + P * 1024);
            so_wo = (unsigned)((8 * hp + rot2) * 36 * XROWB); so_wend = (unsigned)((8 * hp + 8) * 36 * XROWB);
            asm volatile("" : "+s"(so_p1w), "+s"(so_p1r), "+s"(so_p2w), "+s"(so_p2r), "+s"(so_fl), "+s"(so_o), "+s"(so_wo), "+s"(so_wend));
        }
    };
    auto lo16 = [](unsigned v) { return v & 0xffffu; };
    auto hi16 = [](unsigned v) { return v >> 16; };
    const unsigned l16 = lane * 16;
    setup(IC<9>{});
    setup(IC<0>{});
    sfor<0, 6>([&](auto I_) __attribute__((always_inline)) { setup(IC<3 + decltype(I_)::value>{}); SB(); issue_piece(IC<1>{}, I_); SB(); });
    waitv<7>();
    asm volatile("" : "+v"(gw0), "+v"(wpv[0]), "+v"(wpv[1]), "+v"(wpv[2]), "+v"(wpv[3]), "+v"(wpv[4]), "+v"(ep0), "+v"(cpf0));
    const unsigned tag = __builtin_amdgcn_readfirstlane(ep0) + 1u;
    const unsigned th = tag * 0x9E3779B1u, tb[4] = {th & 0xffu, (th >> 8) & 0xffu, (th >> 16) & 0xffu, th >> 24};
    auto mark = [&](f4 v) { return (u4){(__float_as_uint(v[0]) & ~0xffu) | tb[0], (__float_as_uint(v[1]) & ~0xffu) | tb[1], (__float_as_uint(v[2]) & ~0xffu) | tb[2], (__float_as_uint(v[3]) & ~0xffu) | tb[3]}; };
    *(LAS unsigned*)(uintptr_t)agw = gw0;
    BAR();

    bf8 xf[2][18];
    bf8 wf[2][5], gf[2];
    f4 acc[4][2], accx = {0, 0, 0, 0};
    float gl = 0.f;
#pragma unroll
    for (int T = 0; T < 4; ++T) { acc[T][0] = (f4){0, 0, 0, 0}; acc[T][1] = (f4){0, 0, 0, 0}; }
    unsigned aw;
    auto reads = [&](auto S_, auto T_) {
        constexpr int s = decltype(S_)::value, T = decltype(T_)::value, ch = s >> 1, ks = s & 1;
        if constexpr (T == 0) {
            aw = LB + slot_of(ch) + aw0 + ks * 9216;
            asm volatile("" : "+v"(aw));
            wf[ks][0] = trfrag(aw, 2304); xf[0][s] = ld128(LB + slot_of(ch) + (ax0 ^ (ks * 64))); xf[1][s] = ld128(LB + slot_of(ch) + (ax0 ^ (ks * 64 + 2048)));
        } else if constexpr (T == 1) { wf[ks][1] = trfrag(aw + 32, 2304); wf[ks][2] = trfrag(aw + 144, 2304); }
        else if constexpr (T == 2) {
            wf[ks][3] = trfrag(aw + 176, 2304);
            unsigned a4 = LB + slot_of(ch) + aw4 + ks * 9216;
            asm volatile("" : "+v"(a4));
            wf[ks][4] = trfrag(a4, 2304);
            gf[ks] = ld128(ag + s * 64);
        }
    };
    sfor<0, 5>([&](auto T_) { reads(IC<0>{}, T_); });
    sfor<0, 3>([&](auto I_) { issue_piece(IC<2>{}, I_); });
    *(LAS unsigned short*)(uintptr_t)wa2 = (unsigned short)wpv[0];
#pragma unroll
    for (int k = 1; k < 4; ++k) *(LAS unsigned short*)(uintptr_t)(wa0 + k * wstep) = (unsigned short)wpv[k];
    *(LAS unsigned short*)(uintptr_t)wa1 = (unsigned short)wpv[4];
    sfor<0, 18>([&](auto S_) __attribute__((always_inline)) {
        constexpr int s = decltype(S_)::value, ks = s & 1;
        constexpr int cb = (s + 1) / 2;
        sfor<0, 5>([&](auto T_) __attribute__((always_inline)) {
            constexpr int T = decltype(T_)::value;
            SB();
            if constexpr (T == 0) { __builtin_amdgcn_s_waitcnt(0xC07F); SB(); }
            if constexpr (T < 4) { acc[T][0] = mfma(wf[ks][T], xf[0][s], acc[T][0]); acc[T][1] = mfma(wf[ks][T], xf[1][s], acc[T][1]); }
            else {
                accx = mfma(wf[ks][4], xf[0][s], accx);
#pragma unroll
                for (int j = 0; j < 4; ++j)
                    gl = __builtin_amdgcn_fdot2_f32_bf16((bf2){xf[0][s][2 * j], xf[0][s][2 * j + 1]}, (bf2){gf[ks][2 * j], gf[ks][2 * j + 1]}, gl, false);
            }
            SB();
            if constexpr (T == 0 && s + 1 < 18 && (s + 1) % 2 == 0) {
                waitv<(cb <= 7 ? 6 : 4)>();
                if constexpr (cb == 1) asm volatile("" :: "v"(cpf));
                BAR();
            }
            if constexpr (s + 1 < 18) reads(IC<s + 1>{}, T_);
            {
                constexpr int cn = cb + 2, half = ks ? 0 : 1;
                constexpr int pi = T >= 1 && T <= 3 ? 3 * half + T - 1 : -1;
                if constexpr (cn < 9) { if constexpr (pi >= 0) issue_piece(IC<cn>{}, IC<(pi >= 0 ? pi : 0)>{}); }
                else if constexpr (T == 1 || T == 3) issue_wq(IC<2 * (s - 13) + (T >> 1)>{});
            }
            if constexpr (T == 4 && s >= ARM0 && s < ARM0 + 3) {
                if constexpr (s < ARM0 + 2) gst16_sc1n(l16 + (so_o + (s - ARM0) * 32768), ws, (u4){~0u, ~0u, ~0u, ~0u});
                else gst8_sc1n(vol_st, ws, (u2){~0u, ~0u});
            }
        });
    });
    SB();
    const unsigned xs_own = hi16(pk_x), xs_oth = xs_own + (hp ? -3200 : 3200);
    const unsigned gs = LB + L_KVT + 8192;
    float glogit[2];
    bf4 A1[3];
    auto feature_map = [&](const f4* t0, const f4* t1, bf4 x0, f4 (*P_)[3]) {
        bf8 A0[3];
        const unsigned awt = l16 + hp * 3072;
#pragma unroll
        for (int u = 0; u < 3; ++u) { A0[u] = ld128(LB + awt + L_WT0 + u * 1024); A1[u] = ld64(LB + (awt >> 1) + L_WT1 + u * 512); }
        const bf8 B0 = pack8(t0[0], t1[0]), B1 = pack8(t0[1], t1[1]);
#pragma unroll
        for (int u = 0; u < 3; ++u) P_[0][u] = mfma16(A1[u], x0, mfma(A0[u], B0, (f4){0, 0, 0, 0}));
#pragma unroll
        for (int u = 0; u < 3; ++u) P_[1][u] = mfma(A0[u], B1, (f4){0, 0, 0, 0});
    };
    auto feature_map2 = [&](bf4 x1, f4 (*P_)[3]) {
#pragma unroll
        for (int u = 0; u < 3; ++u) P_[1][u] = mfma16(A1[u], x1, P_[1][u]);
    };
    {
        const bf4 xo = pack4(accx);
        *(LAS bf4*)(uintptr_t)xs_own = xo;
        { f2 t = swap16(gl); gl = t[0] + t[1]; auto r = __builtin_amdgcn_permlane32_swap(__float_as_uint(gl), __float_as_uint(gl), false, false); gl = __uint_as_float(r[0]) + __uint_as_float(r[1]); }
        const unsigned ags = gs + ((l16 >> 2) & 60);
        *(LAS float*)(uintptr_t)(ags + w * 64) = gl;
        glogit[0] = gl;
        const unsigned av = LB + L_V + lo16(pk_v), av1 = av + (hp ? -16 * V_P : 16 * V_P);
        *(LAS bf4*)(uintptr_t)av = pack4(acc[2][0]);
        *(LAS bf4*)(uintptr_t)(av + 32) = pack4(acc[3][0]);
        *(LAS bf4*)(uintptr_t)av1 = pack4(acc[2][1]);
        *(LAS bf4*)(uintptr_t)(av1 + 32) = pack4(acc[3][1]);
        *(LAS u2*)(uintptr_t)(LB + L_V + hi16(pk_v)) = (lane & 16) ? __builtin_bit_cast(u2, xo) : (u2){0x3F80u, 0u};
        f4 Pk[2][3];
        feature_map(acc[0], acc[1], xo, Pk);
        SB(); issue_wq(IC<10>{}); issue_wq(IC<11>{}); SB();
        sfor<0, 2>([&](auto TT_) {
            constexpr int tt = decltype(TT_)::value;
            const int dt = tt ? (hp ? -16 * PHI_P : 16 * PHI_P) : 0;
            const unsigned am = lo16(pk_ph) + dt, at = hi16(pk_ph) + dt;
            unsigned ph[9];
            dual_softmax<true>(Pk[tt][0], Pk[tt][1], Pk[tt][2][0], ph, nullptr, [&]() { SB(); issue_wq(IC<12 + 3 * tt>{}); SB(); }, [&]() {
                SB(); issue_wq(IC<13 + 3 * tt>{});
                if constexpr (tt == 0) {
                    BAR();
                    const bf4 xr = ld64(xs_oth);
                    glogit[1] = *(LAS float*)(uintptr_t)(ags + (w ^ 4) * 64);
                    feature_map2(xr, Pk);
                } else SB();
            });
            *(LAS u4*)(uintptr_t)am = (u4){ph[0], ph[1], ph[2], ph[3]};
            *(LAS u4*)(uintptr_t)(am + 16) = (u4){ph[4], ph[5], ph[6], ph[7]};
            *(LAS unsigned*)(uintptr_t)at = ph[8];
            SB(); issue_wq(IC<14 + 3 * tt>{}); SB();
        });
    }
    const unsigned aF = lo16(pk_F), aF2 = hi16(pk_F), aD1 = lo16(pk_D), aD2 = hi16(pk_D);
    BAR();
    {
        f4 c1 = {0, 0, 0, 0}, c2 = {0, 0, 0, 0}, c3 = {0, 0, 0, 0};
        bf8 F[4], F2[4], D1[4], D2[4];
#pragma unroll
        for (int mm = 0; mm < 4; ++mm) {
            F[mm] = trfrag(aF + mm * 32 * PHI_P, PHI_P); D1[mm] = trfrag(aD1 + mm * 32 * V_P, V_P);
            D2[mm] = trfrag(aD2 + mm * 32 * V_P, V_P); F2[mm] = trfrag(aF2 + mm * 32 * PHI_P, PHI_P);
        }
        SB();
#pragma unroll
        for (int mm = 0; mm < 4; ++mm) { c1 = mfma(F[mm], D1[mm], c1); c2 = mfma(F[mm], D2[mm], c2); c3 = mfma(F2[mm], D1[mm], c3); }
        const f4 ca = fh ? c2 : c1, cb = fh ? c1 : c2;
        *(LAS f4*)(uintptr_t)lo16(pk_st) = cb;
        *(LAS f4*)(uintptr_t)hi16(pk_st) = c3;
        gst16_sc1n(l16 + so_p1w, ws, mark(ca));
        const f4 sm = *(LAS f4*)(uintptr_t)lo16(pk_x);
        gst16_sc1n(lo16(pk_s) + so_p1w, ws, mark(sm));
    }
    waitv<2>();
    gst4_sc1n(so_fl, ws, tag);
    unsigned aq = LB + L_WQ + 9 * 4608 + lo16(pk_q);
    unsigned aq8 = LB + L_WQ + 9 * 4608 + hi16(pk_q);
    asm volatile("" : "+v"(aq), "+v"(aq8));
    BAR();
    unsigned vm, vl;
    auto make_vm = [&]() { vm = lo16(pk_wo); vl = hi16(pk_wo) << 4; asm volatile("" : "+v"(vm), "+v"(vl)); };
    unsigned vwo;
    auto issue_wo = [&](auto U_) {
        constexpr int u = decltype(U_)::value, j = u >> 1, kp = u & 1;
        if constexpr (kp == 0) {
            if constexpr (j < 8) { vwo = vm + so_wo; so_wo += 36 * XROWB; so_wo = so_wo == so_wend ? so_wo - 8 * 36 * XROWB : so_wo; }
            else vwo = vl + hp * 288 * XROWB;
        }
        dma16<wo_addr(u)>(vwo, kp ? wob : woa, lw1024);
    };
    f4 qa[2][2], qx = {0, 0, 0, 0};
#pragma unroll
    for (int T = 0; T < 2; ++T) { qa[T][0] = (f4){0, 0, 0, 0}; qa[T][1] = (f4){0, 0, 0, 0}; }
    constexpr int NWO1 = NEARLY + 2 * (18 - HOP1_CHECK);
    {
        bf8 wqf[2][3];
        auto qreads = [&](auto S_, auto T_) {
            constexpr int s = decltype(S_)::value, T = decltype(T_)::value;
            if constexpr (s == 9 && T == 0) { aq -= 9 * 4608; aq8 -= 9 * 4608; asm volatile("" : "+v"(aq), "+v"(aq8)); }
            constexpr int o = ((17 - s) % 9) * 4608;
            if constexpr (T < 2) wqf[s & 1][T] = trfrag(aq + o + T * 32, 144); else wqf[s & 1][2] = trfrag(aq8 + o, 144);
        };
        sfor<0, 3>([&](auto T_) { qreads(IC<0>{}, T_); });
        const unsigned vQ = (lane < 48 ? l16 : 0) + so_p1r;
        u4 a0, b0;
        auto hop1_load = [&]() {
            a0 = gld16_sc1n(vQ, ws); b0 = gld16_sc1n(vQ + P1_UNIT, ws);
        };
        sfor<0, 18>([&](auto S_) {
            constexpr int s = decltype(S_)::value;
            if constexpr (s == 1) make_vm();
            if constexpr (s == HOP1_ISSUE) hop1_load();
            if constexpr (s == HOP1_CHECK) {
                asm volatile("s_waitcnt vmcnt(0)" : "+v"(a0), "+v"(b0) :: "memory");
                auto late = [&]() { return __any((tagdiff(b0[3], tb[3], tagdiff(b0[2], tb[2], tagdiff(b0[1], tb[1], tagdiff(b0[0], tb[0], tagdiff(a0[3], tb[3], tagdiff(a0[2], tb[2], tagdiff(a0[1], tb[1], a0[0] ^ tb[0]))))))) & 0xffu) != 0); };
                if (__builtin_expect(late(), 0)) {
                    unsigned spins = 0;
                    do { __builtin_amdgcn_s_sleep(1); hop1_load(); asm volatile("s_waitcnt vmcnt(0)" : "+v"(a0), "+v"(b0) :: "memory"); } while (late() && ++spins < SPIN_CAP);
                }
                const f4 sum = __builtin_bit_cast(f4, a0 & ~0xffu) + __builtin_bit_cast(f4, b0 & ~0xffu);
                unsigned ared = LB + L_RED + l16;
                asm volatile("" : "+v"(ared));
                *(LAS f4*)(uintptr_t)(ared + w * 1024) = sum;
                BAR();
                if (w == 0) {
                    f4 r[8];
#pragma unroll
                    for (int i = 0; i < 8; ++i) r[i] = *(LAS f4*)(uintptr_t)(ared + i * 1024);
                    const f4 t = ((r[0] + r[1]) + (r[2] + r[3])) + ((r[4] + r[5]) + (r[6] + r[7]));
                    gst16_sc1n(vQ - so_p1r + so_p2w, ws, (u4){pack2(t[0], t[1]), tag, pack2(t[2], t[3]), tag});
                }
            }
            sfor<0, 3>([&](auto T_) {
                constexpr int T = decltype(T_)::value;
                SB();
                if constexpr (T < 2) { qa[T][0] = mfma(wqf[s & 1][T], xf[0][s], qa[T][0]); qa[T][1] = mfma(wqf[s & 1][T], xf[1][s], qa[T][1]); }
                else qx = mfma(wqf[s & 1][2], xf[0][s], qx);
                SB();
                if constexpr (s + 1 < 18) qreads(IC<s + 1>{}, T_);
                if constexpr (s >= ES && s < ES + NEARLY && T == 0) issue_wo(IC<s - ES>{});
                if constexpr (s >= HOP1_CHECK && T < 2 && NEARLY + 2 * (s - HOP1_CHECK) + T < NWO1) issue_wo(IC<NEARLY + 2 * (s - HOP1_CHECK) + T>{});
            });
        });
    }
    SB();
    unsigned phq[2][9], akh, ak, akh2, ak2;
    float qs[2];
    {
        const bf4 xo = pack4(qx);
        *(LAS bf4*)(uintptr_t)(LB + L_XQ + w * 512 + lane * 8) = xo;
        const unsigned vQ0 = l16 + so_p2r, vQ1 = (l16 & 511) + (so_p2r + 1024);
        u4 a0, a1; unsigned af;
        const unsigned fo = hi16(pk_s) + (unsigned)WS_FLAG;
        {
            f4 Pq[2][3];
            feature_map(qa[0], qa[1], xo, Pq);
            static_assert(NWO1 + 3 <= 12, "these units must not need the end-of-stage-2 barrier");
            SB(); sfor<NWO1, NWO1 + 2>([&](auto G_) { issue_wo(G_); }); SB();
            dual_softmax<false>(Pq[0][0], Pq[0][1], Pq[0][2][0], phq[0], &qs[0], [&]() { SB(); issue_wo(IC<NWO1 + 2>{}); SB(); }, [&]() {
                SB();
                BAR();
                const bf4 xr = ld64(LB + L_XQ + (w ^ 4) * 512 + lane * 8);
                issue_wo(IC<NWO1 + 3>{});
                feature_map2(xr, Pq);
            });
            SB();
            a0 = gld16_sc1n(vQ0, ws); a1 = gld16_sc1n(vQ1, ws); af = gld4_sc1n(fo, ws);
            SB();
            dual_softmax<false>(Pq[1][0], Pq[1][1], Pq[1][2][0], phq[1], &qs[1], [&]() {}, [&]() {});
            SB();
        }
        unsigned ak0 = lo16(pk_k01), ak1 = hi16(pk_k01);
#pragma unroll
        for (int tt = 0; tt < 2; ++tt) {
            const float gt = __builtin_amdgcn_rcpf(1.f + __builtin_amdgcn_exp2f(-1.4426950408889634f * glogit[tt]));
            glogit[tt] = br ? 1.f - gt : gt;
        }
        akh = lo16(pk_kh); ak = hi16(pk_kh);
        akh2 = akh + ((l16 >> 3) & 32); ak2 = ak + ((l16 >> 3) & 32);
        asm volatile("" : "+v"(ak0), "+v"(ak1), "+v"(glogit[0]), "+v"(glogit[1]), "+v"(akh), "+v"(ak), "+v"(akh2), "+v"(ak2));
        asm volatile("" : "+v"(phq[0][0]), "+v"(phq[0][1]), "+v"(phq[0][2]), "+v"(phq[0][3]), "+v"(phq[0][4]), "+v"(phq[0][5]), "+v"(phq[0][6]), "+v"(phq[0][7]), "+v"(phq[0][8]));
        asm volatile("" : "+v"(phq[1][0]), "+v"(phq[1][1]), "+v"(phq[1][2]), "+v"(phq[1][3]), "+v"(phq[1][4]), "+v"(phq[1][5]), "+v"(phq[1][6]), "+v"(phq[1][7]), "+v"(phq[1][8]));
        asm volatile("s_waitcnt vmcnt(0)" : "+v"(a0), "+v"(a1), "+v"(af) :: "memory");
        auto late = [&]() { return __any(tagdiff(af, tag, tagdiff(a1[3], tag, tagdiff(a1[1], tag, tagdiff(a0[3], tag, a0[1] ^ tag)))) != 0); };
        if (__builtin_expect(late(), 0)) {
            unsigned spins = 0;
            do {
                __builtin_amdgcn_s_sleep(1);
                a0 = gld16_sc1n(vQ0, ws); a1 = gld16_sc1n(vQ1, ws); af = gld4_sc1n(fo, ws);
                asm volatile("s_waitcnt vmcnt(0)" : "+v"(a0), "+v"(a1), "+v"(af) :: "memory");
            } while (late() && ++spins < SPIN_CAP);
        }
        *(LAS u2*)(uintptr_t)ak0 = (u2){a0[0], a0[2]};
        *(LAS u2*)(uintptr_t)ak1 = (u2){a1[0], a1[2]};
        sfor<NWO1 + 4, 17>([&](auto G_) { issue_wo(G_); });
        BAR();
        issue_wo(IC<17>{});
    }
    unsigned au[4];
    {
        bf8 A[3][2]; bf4 At[3];
#pragma unroll
        for (int u = 0; u < 3; ++u) {
            A[u][0] = ld128(u < 2 ? ak2 + u * 320 : akh2 + 1280); A[u][1] = ld128((u < 2 ? ak2 + u * 320 : akh2 + 1280) + 16);
            At[u] = ld64((u < 2 ? ak + u * 320 : akh + 1280) + 64);
        }
        unsigned left[2];
#pragma unroll
        for (int tt = 0; tt < 2; ++tt) {
            const bf8 B0 = __builtin_bit_cast(bf8, (u4){phq[tt][0], phq[tt][1], phq[tt][2], phq[tt][3]});
            const bf8 B1 = __builtin_bit_cast(bf8, (u4){phq[tt][4], phq[tt][5], phq[tt][6], phq[tt][7]});
            const bf4 B2 = __builtin_bit_cast(bf4, (lane & 16) ? (u2){0u, phq[tt][8]} : (u2){phq[tt][8], 0u});
            f4 nu[3];
#pragma unroll
            for (int u = 0; u < 3; ++u) nu[u] = mfma16(At[u], B2, mfma(A[u][1], B1, mfma(A[u][0], B0, (f4){0, 0, 0, 0})));
            const float sc = glogit[tt] * __builtin_amdgcn_rcpf(__builtin_fmaf(qs[tt], 1e-6f, nu[2][2]));
            gst16_sc1n(l16 + (so_o + (tt ^ hp) * 32768), ws, __builtin_bit_cast(u4, pack8(nu[0] * sc, nu[1] * sc)));
            left[tt] = pack2(nu[2][0] * sc, nu[2][1] * sc);
        }
        {
            auto r = __builtin_amdgcn_permlane32_swap(left[0], left[1], false, false);
            gst8_sc1n(vol_st, ws, (u2){r[0], r[1]});
        }
#pragma unroll
        for (int u = 0; u < 4; ++u) au[u] = lo16(pk_au) ^ (u * 32);
        asm volatile("" : "+v"(au[0]), "+v"(au[1]), "+v"(au[2]), "+v"(au[3]));
        waitv<3>();
    }
    BAR();
    if (has2) {
        const char* obr = ws + WS_O + (size_t)kq2 * 8 * 1024;
        const char* ol = ws + WS_OL + (size_t)kq2 * 8 * 128;
        const int ntl = 3 - mh;
        unsigned vo[3], vol[3];
#pragma unroll
        for (int t = 0; t < 3; ++t) { const int T = T0 + t < 127 ? T0 + t : 127; vo[t] = T * 32768 + l16; vol[t] = T * 4096 + hi16(pk_au); }
        const int last_off = kq2 < 2 ? wo_addr(16) : wo_addr(17) - 8192;
        const unsigned ascr = LB + (kq2 * 5 + 3 * mh) * 4352 + g * 272 + c * 16;
        unsigned ard = LB + 3 * mh * 4352 + c * 272 + (4 * kq2 + g) * 16;
        unsigned vout = (unsigned)((T0 * 16 + 4 * kq2 + g) * XROWB + nt * 128 + c * 8);
        asm volatile("" : "+v"(ard), "+v"(vout));
        unsigned tries = 0;
        auto gemm2 = [&](auto NT_) {
            constexpr int NTL = decltype(NT_)::value, NG = 3 * NTL;
            u4 of[OBUF][3]; u2 ofl2[2][2];
            auto oload = [&](auto E_) {
                constexpr int e = decltype(E_)::value, t = e / 3, gi = e % 3;
#pragma unroll
                for (int i = 0; i < (gi < 2 ? 3 : 2); ++i) of[e % OBUF][i] = gld16_sc1n(vo[t], obr + ((3 * gi + i + rot2) & 7) * 1024);
                if constexpr (gi == 2) { ofl2[t & 1][0] = gld8_sc1o<0>(vol[t], ol); ofl2[t & 1][1] = gld8_sc1o<128>(vol[t], ol); }
            };
            bf8 wr[9][4];
            unsigned ab[4];
            auto wread = [&](auto G_) {
                sfor<3 * decltype(G_)::value, 3 * decltype(G_)::value + 3>([&](auto J_) {
                    constexpr int j = decltype(J_)::value;
#pragma unroll
                    for (int u = 0; u < 4; ++u) {
                        if constexpr (j == 3 || j == 6 || j == 8) { ab[u] = au[u] + (j < 8 ? wo_addr(2 * j) : last_off); asm volatile("" : "+v"(ab[u])); }
                        wr[j][u] = j < 3 ? trfrag(au[u] + wo_addr(2 * j), 1024) : trfrag(ab[u] + (j == 8 ? 0 : wo_addr(2 * j) - wo_addr(2 * (j < 6 ? 3 : 6))), 1024);
                    }
                });
            };
            sfor<0, 3>([&](auto G_) { wread(G_); });
            SB(); sfor<0, OBUF - 1>([&](auto E_) { oload(E_); }); SB();
            auto gwait = [&](auto E_, auto N_) {
                constexpr int e = decltype(E_)::value, n = decltype(N_)::value;
                if constexpr (e % 3 < 2) waitr<n>(of[e % OBUF][0], of[e % OBUF][1], of[e % OBUF][2]);
                else { waitr<n>(of[e % OBUF][0], of[e % OBUF][1]); waitr<n>(ofl2[(e / 3) & 1][0], ofl2[(e / 3) & 1][1]); }
            };
            f4 oc[4];
            sfor<0, NG>([&](auto E_) {
                constexpr int e = decltype(E_)::value, t = e / 3, gi = e % 3;
                SB();
                if constexpr (e + OBUF - 1 < NG) oload(IC<e + OBUF - 1>{});
                if constexpr (e == 0) BAR();
                constexpr auto cnt = [](int k) { return k < NG ? (k % 3 == 2 ? 4 : 3) : 0; };
                constexpr int ahead = cnt(e + 1) + (OBUF > 2 ? cnt(e + 2) : 0) + (OBUF > 3 ? cnt(e + 3) : 0);
                auto frag = [&](int i) { if (gi < 2 || i < 2) return of[e % OBUF][i]; else return (u4){ofl2[t & 1][0][0], ofl2[t & 1][0][1], ofl2[t & 1][1][0], ofl2[t & 1][1][1]}; };
                auto missing = [&]() {
                    unsigned mx = 0;
#pragma unroll
                    for (int i = 0; i < 3; ++i) { const u4 bb = frag(i); mx = max(max(mx, bb[0]), bb[1]); mx = max(max(mx, bb[2]), bb[3]); }
                    return __any(mx == ~0u);
                };
                gwait(E_, IC<ahead>{});
                if (__builtin_expect(missing(), 0)) {
                    waitv<0>();
                    constexpr int eh = e + OBUF < NG ? e + OBUF : NG;
                    do {
                        __builtin_amdgcn_s_sleep(1);
                        sfor<e, eh>([&](auto F_) { oload(F_); });
                        sfor<e, eh>([&](auto F_) { gwait(F_, IC<0>{}); });
                    } while (missing() && ++tries < SPIN_CAP);
                }
#pragma unroll
                for (int i = 0; i < 3; ++i) {
                    const bf8 B = __builtin_bit_cast(bf8, frag(i));
#pragma unroll
                    for (int u = 0; u < 4; ++u) oc[u] = mfma(wr[3 * gi + i][u], B, (gi == 0 && i == 0) ? (f4){0, 0, 0, 0} : oc[u]);
                }
                if constexpr (gi == 2) {
#pragma unroll
                    for (int u = 0; u < 4; ++u) *(LAS f4*)(uintptr_t)(ascr + t * 4352 + u * 1088) = oc[u];
                }
            });
            SB();
        };
        if (mh == 0) gemm2(IC<3>{});
        else gemm2(IC<2>{});
        BAR();
        {
            f4 r[3][4];
#pragma unroll
            for (int t = 0; t < 3; ++t)
#pragma unroll
                for (int sq = 0; sq < 4; ++sq) r[t][sq] = *(LAS f4*)(uintptr_t)(ard + sq * 21760 + t * 4352);
#pragma unroll
            for (int t = 0; t < 3; ++t) {
                const f4 v = (r[t][0] + r[t][1]) + (r[t][2] + r[t][3]);
                if (t < ntl && T0 + t < 128) gst8_sc1n(vout + t * 16 * XROWB, out, __builtin_bit_cast(u2, pack4(v)));
            }
        }
        if (b == 0 && tid == 0) gst4_sc1(0, ws + WS_EPOCH, tag);
    }
}

__device__ __attribute__((used, noinline)) void kf_code_end() { asm volatile("Lkf_end:"); }

extern "C" void kernel_launch(void* const* d_in, const int* in_sizes, int n_in,
                              void* d_out, int out_size, void* d_ws, size_t ws_size,
                              hipStream_t stream) {
    kf<<<256, 512, 0, stream>>>((const char*)d_in[0], (const char*)d_in[1], (const char*)d_in[5], (const char*)d_in[2], (const char*)d_in[6],
                                (const char*)d_in[8], (const char*)d_in[3], (const char*)d_in[7], (char*)d_ws, (char*)d_out);
}
```
